```python
import torch

def _softmax(x, axis=-1):
    return torch.softmax(x, dim=axis)
def _gelu(x):
    return torch.nn.functional.gelu(x, approximate="tanh")
def _silu(x):
    return torch.nn.functional.silu(x)
def _sigmoid(x):
    return torch.sigmoid(x)
def _layernorm(x, axis=-1, eps=1e-5):
    m = x.mean(dim=axis, keepdim=True)
    v = x.var(dim=axis, keepdim=True, unbiased=False)
    return (x - m) / torch.sqrt(v + eps)
def _scatter_add(tgt, idx, src, axis=0):
    return tgt.clone().index_add_(axis, idx, src)
def _segment_sum(src, seg, n):
    out = torch.zeros((n,) + src.shape[1:], dtype=src.dtype)
    return out.index_add_(0, seg, src)
def _pad(x, pw):
    flat = tuple(v for pair in reversed(pw) for v in pair)
    return torch.nn.functional.pad(x, flat)

kernel_name = 'parallel_sum__sliding_block_cached__glu_gated_geglu__decode'

def setup_inputs():

    _w = (lambda a: (lambda t: t.to(torch.bfloat16) if t.is_floating_point() else t)(torch.as_tensor(a)))
    def _A_setup():
        B, Sq, D = 2, 1, 640
        Hq, Hkv, Sk, Dh = 32, 32, 8192, 192
        assert Hq % Hkv == 0, (Hq, Hkv)
        return {
            "x": _w((lambda *s: torch.randn(*s, dtype=torch.bfloat16) * 0.05)(B, Sq, D)),
            "k_cache": _w((lambda *s: torch.randn(*s, dtype=torch.bfloat16) * 1.6)(B, Hkv, Sk, Dh)),
            "v_cache": _w((lambda *s: torch.randn(*s, dtype=torch.bfloat16) * 0.8)(B, Hkv, Sk, Dh)),
            "wq": _w((lambda *s: torch.randn(*s, dtype=torch.bfloat16) * 1.6)(D, Hq * Dh) * (D ** -0.5)),
            "wo": _w((lambda *s: torch.randn(*s, dtype=torch.bfloat16) * 0.8)(Hq * Dh, D) * ((Hq * Dh) ** -0.5)),
        }
    _A_in = _A_setup()

    def _B_setup():
        B, S, Dm, Df = 2, 1, 640, 1536
        return {
            "x": _w((lambda *s: torch.randn(*s, dtype=torch.bfloat16) * 0.05)(B, S, Dm)),
            "w_gate": _w((lambda *s: torch.randn(*s, dtype=torch.bfloat16) * 0.8)(Dm, Df) * (Dm ** -0.5)),
            "w_up": _w((lambda *s: torch.randn(*s, dtype=torch.bfloat16) * 0.8)(Dm, Df) * (Dm ** -0.5)),
            "w_down": _w((lambda *s: torch.randn(*s, dtype=torch.bfloat16) * 0.8)(Df, Dm) * (Df ** -0.5)),
        }
    _B_in = _B_setup()

    return {
        "A_x": _A_in["x"],
        "A_k_cache": _A_in["k_cache"],
        "A_v_cache": _A_in["v_cache"],
        "A_wq": _A_in["wq"],
        "A_wo": _A_in["wo"],
        "B_x": _B_in["x"],
        "B_w_gate": _B_in["w_gate"],
        "B_w_up": _B_in["w_up"],
        "B_w_down": _B_in["w_down"],
    }

def reference(A_x, A_k_cache, A_v_cache, A_wq, A_wo, B_x, B_w_gate, B_w_up, B_w_down):
    def _A_ref(x, k_cache, v_cache, wq, wo):
        B, Sq, D = x.shape
        Hq, Dh = 32, 192
        Hkv, Sk = k_cache.shape[1], k_cache.shape[2]
        W = min(4096, max(256, Sk // 4))
        rep = Hq // Hkv
        q = (lambda x, *d: x.permute(*d))((x @ wq).reshape(B, Sq, Hq, Dh), 0, 2, 1, 3)
        k = (lambda x, n, axis: torch.repeat_interleave(x, n, dim=axis))(k_cache[:, :, Sk - W:, :], rep, axis=1)
        v = (lambda x, n, axis: torch.repeat_interleave(x, n, dim=axis))(v_cache[:, :, Sk - W:, :], rep, axis=1)
        scores = torch.einsum("bhqd,bhkd->bhqk", q, k) * (Dh ** -0.5)
        p = _softmax(scores, axis=-1)
        o = torch.einsum("bhqk,bhkd->bhqd", p, v)
        return (lambda x, *d: x.permute(*d))(o, 0, 2, 1, 3).reshape(B, Sq, Hq * Dh) @ wo

    def _B_ref(x, w_gate, w_up, w_down):
        g = torch.einsum("bsd,df->bsf", x, w_gate)
        u = torch.einsum("bsd,df->bsf", x, w_up)
        h = _gelu(g) * u
        return torch.einsum("bsf,fd->bsd", h, w_down)

    x = A_x
    xn = _layernorm(x, axis=-1)
    return x + _A_ref(xn, A_k_cache, A_v_cache, A_wq, A_wo) + _B_ref(xn, B_w_gate, B_w_up, B_w_down)
```

```cpp
#include <hip/hip_runtime.h>
#include <hip/hip_bf16.h>
#include <stdint.h>

typedef unsigned v4u __attribute__((ext_vector_type(4)));
typedef unsigned short u16;
constexpr int B = 2, D = 640, H = 32, SK = 8192, DH = 192, W = 2048, DF = 1536;
__device__ __forceinline__ float bf2f(u16 b) { return __builtin_bit_cast(float, (unsigned)b << 16); }
__device__ __forceinline__ float bflo(unsigned u) { return __builtin_bit_cast(float, u << 16); }
__device__ __forceinline__ float bfhi(unsigned u) { return __builtin_bit_cast(float, u & 0xffff0000u); }
__device__ __forceinline__ float rbf(float f) { return (float)(__bf16)f; }

__device__ void layernorm(const u16* x, float* xn  , float* red) {
  const int tid = threadIdx.x;
  const int w = tid >> 6, lane = tid & 63;
  if (w < 2) {
    float v[10]; float s = 0.f;
    for (int i = 0; i < 10; ++i) { v[i] = bf2f(x[w * D + lane + 64 * i]); s += v[i]; }
    for (int o = 32; o; o >>= 1) s += __shfl_xor(s, o);
    float m = s / D; float q = 0.f;
    for (int i = 0; i < 10; ++i) { float d = v[i] - m; q += d * d; }
    for (int o = 32; o; o >>= 1) q += __shfl_xor(q, o);
    float r = 1.f / sqrtf(q / D + 1e-5f);
    for (int i = 0; i < 10; ++i) xn[w * D + lane + 64 * i] = rbf((v[i] - m) * r);
  }
  __syncthreads();
}

__global__ void __launch_bounds__(256) k1(const u16* x, const u16* wq, const u16* wg, const u16* wu, float* q, float* hbuf) {
  __shared__ float xn[2 * D];
  __shared__ float red[32 * 2 * 2 * 64];
  layernorm(x, xn, red);
  const int tid = threadIdx.x, e = tid & 7, r = tid >> 3;
  const int blk = blockIdx.x;
  const bool isq = blk < 96;
  const int nm = isq ? 1 : 2;
  for (int m = 0; m < nm; ++m) {
    const u16* wp = isq ? wq + blk * 64 : (m == 0 ? wg : wu) + (blk - 96) * 64;
    const int ld = isq ? H * DH : DF;
    float a0[8] = {0}, a1[8] = {0};
    for (int k = r; k < D; k += 32) {
      v4u v = __builtin_nontemporal_load((const v4u*)(wp + (size_t)k * ld + e * 8));
      float x0 = xn[k], x1 = xn[D + k];
      for (int j = 0; j < 4; ++j) {
        a0[2 * j] += x0 * bflo(v[j]); a0[2 * j + 1] += x0 * bfhi(v[j]);
        a1[2 * j] += x1 * bflo(v[j]); a1[2 * j + 1] += x1 * bfhi(v[j]);
      }
    }
    for (int j = 0; j < 8; ++j) { red[((r * 2 + m) * 2 + 0) * 64 + e * 8 + j] = a0[j]; red[((r * 2 + m) * 2 + 1) * 64 + e * 8 + j] = a1[j]; }
  }
  __syncthreads();
  if (tid < 128) {
    const int b = tid >> 6, c = tid & 63;
    float s0 = 0.f, s1 = 0.f;
    for (int rr = 0; rr < 32; ++rr) { s0 += red[((rr * 2 + 0) * 2 + b) * 64 + c]; if (!isq) s1 += red[((rr * 2 + 1) * 2 + b) * 64 + c]; }
    if (isq) q[b * H * DH + blk * 64 + c] = rbf(s0);
    else {
      float g = s0, u = s1;
      float t = 0.7978845608028654f * (g + 0.044715f * g * g * g);
      float ge = 0.5f * g * (1.f + tanhf(t));
      hbuf[b * DF + (blk - 96) * 64 + c] = ge * u;
    }
  }
}

__global__ void __launch_bounds__(256) k2(const u16* K, const u16* V, const float* q, float* part) {
  __shared__ float sm[4][200];
  __shared__ float ps[4][64];
  const int bh = blockIdx.x >> 3, s = blockIdx.x & 7;
  const int tid = threadIdx.x, w = tid >> 6, lane = tid & 63, a = lane >> 3, e = lane & 7;
  const size_t base = ((size_t)bh * SK + (SK - W) + s * 256 + w * 64) * DH;
  const int b = bh >> 5, h = bh & 31;
  float qv[24];
  for (int c = 0; c < 3; ++c) for (int j = 0; j < 8; ++j) qv[c * 8 + j] = q[b * H * DH + h * DH + c * 64 + e * 8 + j];
  float sc = 0.f;
  for (int t = 0; t < 8; ++t) {
    float d = 0.f;
    for (int c = 0; c < 3; ++c) {
      v4u v = __builtin_nontemporal_load((const v4u*)(K + base + (size_t)(t * 8 + a) * DH + c * 64 + e * 8));
      for (int j = 0; j < 4; ++j) d += bflo(v[j]) * qv[c * 8 + 2 * j] + bfhi(v[j]) * qv[c * 8 + 2 * j + 1];
    }
    d += __shfl_xor(d, 1); d += __shfl_xor(d, 2); d += __shfl_xor(d, 4);
    if (e == t) sc = d;
  }
  sc *= 0.07216878364870323f;
  float m = sc;
  for (int o = 32; o; o >>= 1) m = fmaxf(m, __shfl_xor(m, o));
  float p = __expf(sc - m), l = p;
  for (int o = 32; o; o >>= 1) l += __shfl_xor(l, o);
  float acc[24] = {0};
  for (int t = 0; t < 8; ++t) {
    float pt = __shfl(p, a * 8 + t);
    for (int c = 0; c < 3; ++c) {
      v4u v = __builtin_nontemporal_load((const v4u*)(V + base + (size_t)(t * 8 + a) * DH + c * 64 + e * 8));
      for (int j = 0; j < 4; ++j) { acc[c * 8 + 2 * j] += pt * bflo(v[j]); acc[c * 8 + 2 * j + 1] += pt * bfhi(v[j]); }
    }
  }
  for (int i = 0; i < 24; ++i) { acc[i] += __shfl_xor(acc[i], 8); acc[i] += __shfl_xor(acc[i], 16); acc[i] += __shfl_xor(acc[i], 32); }
  if (a == 0) for (int c = 0; c < 3; ++c) for (int j = 0; j < 8; ++j) sm[w][c * 64 + e * 8 + j] = acc[c * 8 + j];
  if (lane == 0) { sm[w][192] = m; sm[w][193] = l; }
  __syncthreads();
  if (tid < 192) {
    float M = fmaxf(fmaxf(sm[0][192], sm[1][192]), fmaxf(sm[2][192], sm[3][192]));
    float o = 0.f, L = 0.f;
    for (int ww = 0; ww < 4; ++ww) { float f = __expf(sm[ww][192] - M); o += f * sm[ww][tid]; L += f * sm[ww][193]; }
    float* pp = part + (size_t)blockIdx.x * 256;
    pp[tid] = o;
    if (tid == 0) { pp[192] = M; pp[193] = L; }
  }
}

__global__ void __launch_bounds__(256) k3(const float* part, const float* hbuf, const u16* wo, const u16* wd, float* p2) {
  __shared__ float ov[2][320];
  __shared__ float red[32][2][64];
  const int rg = blockIdx.x / 10, cg = blockIdx.x % 10, tid = threadIdx.x;
  for (int i = tid; i < 640; i += 256) {
    const int b = i / 320, r = rg * 320 + i % 320;
    float val;
    if (r < H * DH) {
      const int h = r / DH, d = r % DH;
      const float* pp = part + (size_t)((b * 32 + h) * 8) * 256;
      float M = -1e30f;
      for (int s = 0; s < 8; ++s) M = fmaxf(M, pp[s * 256 + 192]);
      float o = 0.f, L = 0.f;
      for (int s = 0; s < 8; ++s) { float f = __expf(pp[s * 256 + 192] - M); o += f * pp[s * 256 + d]; L += f * pp[s * 256 + 193]; }
      val = o / L;
    } else val = hbuf[b * DF + r - H * DH];
    ov[b][i % 320] = val;
  }
  __syncthreads();
  const int e = tid & 7, r = tid >> 3;
  float a0[8] = {0}, a1[8] = {0};
  for (int k = r; k < 320; k += 32) {
    const int row = rg * 320 + k;
    const u16* wp = row < H * DH ? wo + (size_t)row * D : wd + (size_t)(row - H * DH) * D;
    v4u v = __builtin_nontemporal_load((const v4u*)(wp + cg * 64 + e * 8));
    float x0 = ov[0][k], x1 = ov[1][k];
    for (int j = 0; j < 4; ++j) {
      a0[2 * j] += x0 * bflo(v[j]); a0[2 * j + 1] += x0 * bfhi(v[j]);
      a1[2 * j] += x1 * bflo(v[j]); a1[2 * j + 1] += x1 * bfhi(v[j]);
    }
  }
  for (int j = 0; j < 8; ++j) { red[r][0][e * 8 + j] = a0[j]; red[r][1][e * 8 + j] = a1[j]; }
  __syncthreads();
  if (tid < 128) {
    const int b = tid >> 6, c = tid & 63;
    float s = 0.f;
    for (int rr = 0; rr < 32; ++rr) s += red[rr][b][c];
    p2[(size_t)rg * 1280 + b * 640 + cg * 64 + c] = s;
  }
}

__global__ void __launch_bounds__(256) k4(const float* p2, const u16* x, __hip_bfloat16* out) {
  const int i = blockIdx.x * 256 + threadIdx.x;
  if (i < 1280) {
    float s = bf2f(x[i]);
    for (int r = 0; r < 24; ++r) s += p2[r * 1280 + i];
    out[i] = __float2bfloat16(s);
  }
}

extern "C" void kernel_launch(void* const* d_in, const int* in_sizes, int n_in,
                              void* d_out, int out_size, void* d_ws, size_t ws_size,
                              hipStream_t stream) {
  const u16* x = (const u16*)d_in[0];
  const u16* K = (const u16*)d_in[1];
  const u16* V = (const u16*)d_in[2];
  const u16* wq = (const u16*)d_in[3];
  const u16* wo = (const u16*)d_in[4];
  const u16* wg = (const u16*)d_in[6];
  const u16* wu = (const u16*)d_in[7];
  const u16* wd = (const u16*)d_in[8];
  float* ws = (float*)d_ws;
  float* q = ws;
  float* hb = ws + 16384;
  float* part = ws + 32768;
  float* p2 = part + 512 * 256;
  k1<<<120, 256, 0, stream>>>(x, wq, wg, wu, q, hb);
  k2<<<512, 256, 0, stream>>>(K, V, q, part);
  k3<<<240, 256, 0, stream>>>(part, hb, wo, wd, p2);
  k4<<<5, 256, 0, stream>>>(p2, x, (__hip_bfloat16*)d_out);
}
```

```cpp
#include <hip/hip_runtime.h>
#include <hip/hip_bf16.h>
#include <stdint.h>

namespace {
typedef unsigned v4u __attribute__((ext_vector_type(4)));
typedef unsigned v2u __attribute__((ext_vector_type(2)));
typedef float f4 __attribute__((ext_vector_type(4)));
typedef float f2 __attribute__((ext_vector_type(2)));
typedef __bf16 bf8 __attribute__((ext_vector_type(8)));
typedef __bf16 bf2 __attribute__((ext_vector_type(2)));
typedef unsigned short u16;
#define GAS __attribute__((address_space(1)))

constexpr int D = 640, NH = 32, SK = 8192, DH = 192, WIN = 2048, DF = 1536;
constexpr int NT = 2, SC1 = 16;
#define WND 4
#define STAMP 0
constexpr int W = WND;
constexpr int WS_E = 0;
constexpr int WS_QP = 4096;
constexpr int WS_GU = WS_QP + 10 * 2 * 3072 * 16;
constexpr int WS_ST = WS_GU + 10 * 2 * 1536 * 16;
constexpr int WS_OP = WS_ST + 64 * 32 * 16;
constexpr int WS_FP = WS_OP + 2 * 4 * 3072 * 16;
constexpr int WS_END = WS_FP + 48 * 640 * 16;
constexpr int WS_STAMP = 4 << 20;
constexpr unsigned SPIN_CAP = 1u << 16;

__device__ __forceinline__ float asf(unsigned u) { return __builtin_bit_cast(float, u); }
__device__ __forceinline__ unsigned asu(float f) { return __builtin_bit_cast(unsigned, f); }
__device__ __forceinline__ float bflo(unsigned u) { return asf(u << 16); }
__device__ __forceinline__ float bfhi(unsigned u) { return asf(u & 0xffff0000u); }
__device__ __forceinline__ unsigned pk(float lo, float hi) { bf2 r = {(__bf16)lo, (__bf16)hi}; return __builtin_bit_cast(unsigned, r); }
__device__ __forceinline__ float rbf(float f) { return (float)(__bf16)f; }
template <int CTRL> __device__ __forceinline__ float dpp(float x) { return asf(__builtin_amdgcn_mov_dpp(asu(x), CTRL, 0xf, 0xf, true)); }
constexpr int X1 = 0xB1, X2 = 0x4E, HM = 0x141, R8 = 0x128;
__device__ __forceinline__ float sum16(float x) { auto s = __builtin_amdgcn_permlane16_swap(asu(x), asu(x), false, false); return asf(s[0]) + asf(s[1]); }
__device__ __forceinline__ float sum32(float x) { auto s = __builtin_amdgcn_permlane32_swap(asu(x), asu(x), false, false); return asf(s[0]) + asf(s[1]); }
__device__ __forceinline__ float max16(float x) { auto s = __builtin_amdgcn_permlane16_swap(asu(x), asu(x), false, false); return fmaxf(asf(s[0]), asf(s[1])); }
__device__ __forceinline__ float max32(float x) { auto s = __builtin_amdgcn_permlane32_swap(asu(x), asu(x), false, false); return fmaxf(asf(s[0]), asf(s[1])); }
__device__ __forceinline__ float sum8(float x) { x += dpp<X1>(x); x += dpp<X2>(x); x += dpp<HM>(x); return x; }
__device__ __forceinline__ float dot2(unsigned a, unsigned b, float c) { return __builtin_amdgcn_fdot2_f32_bf16(__builtin_bit_cast(bf2, a), __builtin_bit_cast(bf2, b), c, false); }
__device__ __forceinline__ f4 mfma(v4u a, v4u b, f4 c) { return __builtin_amdgcn_mfma_f32_16x16x32_bf16(__builtin_bit_cast(bf8, a), __builtin_bit_cast(bf8, b), c, 0, 0, 0); }
__device__ __forceinline__ v4u afrag(float w, v4u m) { const unsigned d = pk(w, w); return v4u{d & m.x, d & m.y, d & m.z, d & m.w}; }
typedef __amdgpu_buffer_rsrc_t rsrc;
__device__ __forceinline__ rsrc mkr(const void* p, int bytes) { return __builtin_amdgcn_make_buffer_rsrc((void*)p, 0, bytes, 0x00020000); }
#define SB() __builtin_amdgcn_sched_barrier(0)
#define PACE() do { SB(); asm volatile("s_waitcnt vmcnt(%0)" :: "n"(W - 1)); SB(); } while (0)
#define BAR() do { SB(); asm volatile("s_waitcnt lgkmcnt(0)\n\ts_barrier" ::: "memory"); SB(); } while (0)
__device__ __forceinline__ v4u ldws(rsrc r, int off) { return __builtin_amdgcn_raw_buffer_load_b128(r, off, 0, SC1); }
__device__ __forceinline__ void stws(rsrc r, int off, float a, float b, unsigned tag) { __builtin_amdgcn_raw_buffer_store_b128(v4u{asu(a), tag, asu(b), tag}, r, off, 0, SC1); }
__device__ __forceinline__ unsigned bad(v4u p, unsigned tag) { return (p.y ^ tag) | (p.w ^ tag); }
#define AWAIT(BAD, RELOAD) do { if (__builtin_expect(__any((BAD) != 0u), 0)) { unsigned spin_ = 0; \
    _Pragma("nounroll") do { asm volatile("" ::: "memory"); RELOAD; } while (__any((BAD) != 0u) && ++spin_ < SPIN_CAP); } } while (0)
#define TS(i)

__global__ void __launch_bounds__(512, 2) fused(const u16* __restrict__ x, const u16* __restrict__ Kc, const u16* __restrict__ Vc, const u16* __restrict__ wq,
                                                const u16* __restrict__ wo, const u16* __restrict__ wg, const u16* __restrict__ wu, const u16* __restrict__ wd,
                                                u16* __restrict__ out, unsigned char* ws) {
  __shared__ __attribute__((aligned(16))) float red[8 * 768 + 128];
  const int tid = threadIdx.x, lane = tid & 63, w = __builtin_amdgcn_readfirstlane(tid >> 6);
  TS(0);
  const int xg = blockIdx.x & 7, r = blockIdx.x >> 3;
  const int h = xg * 4 + (r >> 3), b = (r >> 2) & 1, sp = r & 3;
  const bool hasA = r >= 2, hasF = r < 30;
  const int ua = r - 2, ks = ua % 10, cgg = xg * 3 + ua / 10;
  const int rg = xg * 6 + r / 5, cg = r % 5;
  const int g4 = lane >> 4, j = lane & 15, lb = (lane >> 3) & 1, e = lane & 7, a = lane >> 3;
  const rsrc rW = mkr(ws, WS_END);
  const rsrc rX = mkr(x, 2 * D * 2);
  const size_t kvb = ((size_t)(b * NH + h) * SK + (SK - WIN) + sp * 512) * DH;
  const rsrc rK = mkr(Kc + kvb, 512 * DH * 2), rV = mkr(Vc + kvb, 512 * DH * 2);
  const int kvo = w * 3072 + a * 384 + e * 16;
#define LDK(i) __builtin_amdgcn_raw_buffer_load_b128(rK, kvo + ((i) % 3) * 128, ((i) / 3) * 24576, NT)
#define LDV(i) __builtin_amdgcn_raw_buffer_load_b128(rV, kvo + ((i) % 3) * 128, ((i) / 3) * 24576, NT)

  const unsigned tag = __builtin_amdgcn_raw_buffer_load_b32(rW, WS_E, 0, SC1) + 1u;
  v4u wa[2][3];
  v4u xs0, xs1; v2u xs2; unsigned xr;
  if (hasA) {
    const int jj = e | (g4 << 3);
    xs0 = __builtin_amdgcn_raw_buffer_load_b128(rX, lb * 1280 + jj * 16, 0, 0);
    xs1 = __builtin_amdgcn_raw_buffer_load_b128(rX, lb * 1280 + 512 + jj * 16, 0, 0);
    xs2 = __builtin_amdgcn_raw_buffer_load_b64(rX, lb * 1280 + 1024 + jj * 8, 0, 0);
    const int row0 = 64 * ks + 8 * w + 2 * g4;
    xr = __builtin_amdgcn_raw_buffer_load_b32(rX, lb * 1280 + row0 * 2, 0, 0);
    const rsrc rQ = mkr(wq + 256 * cgg, D * NH * DH * 2 - 512 * cgg);
    const GAS u16* gu = (const GAS u16*)(lb ? wu : wg) + (size_t)row0 * DF + 64 * cgg + 8 * e;
#pragma unroll
    for (int rq = 0; rq < 2; ++rq) {
      wa[rq][0] = __builtin_amdgcn_raw_buffer_load_b128(rQ, row0 * 12288 + j * 16, rq * 12288, NT);
      wa[rq][1] = __builtin_amdgcn_raw_buffer_load_b128(rQ, row0 * 12288 + j * 16 + 256, rq * 12288, NT);
      wa[rq][2] = __builtin_nontemporal_load((const GAS v4u*)(gu + rq * DF));
    }
  }
  SB();
  v4u kb[24], vb[24];
  kb[0] = LDK(0); kb[1] = LDK(1); kb[2] = LDK(2);
  SB();
  const v4u msk = {(e >> 1) == 0 ? ((e & 1) ? 0xffff0000u : 0xffffu) : 0u, (e >> 1) == 1 ? ((e & 1) ? 0xffff0000u : 0xffffu) : 0u,
                   (e >> 1) == 2 ? ((e & 1) ? 0xffff0000u : 0xffffu) : 0u, (e >> 1) == 3 ? ((e & 1) ? 0xffff0000u : 0xffffu) : 0u};
  if (hasA) {
    float v[20];
#pragma unroll
    for (int i = 0; i < 4; ++i) { v[2 * i] = bflo(xs0[i]); v[2 * i + 1] = bfhi(xs0[i]); v[8 + 2 * i] = bflo(xs1[i]); v[9 + 2 * i] = bfhi(xs1[i]); }
    v[16] = bflo(xs2[0]); v[17] = bfhi(xs2[0]); v[18] = bflo(xs2[1]); v[19] = bfhi(xs2[1]);
    float s = 0.f;
#pragma unroll
    for (int i = 0; i < 20; ++i) s += v[i];
    s = sum32(sum16(sum8(s)));
    const float mean = s * (1.f / D);
    float q = 0.f;
#pragma unroll
    for (int i = 0; i < 20; ++i) { const float d = v[i] - mean; q = fmaf(d, d, q); }
    q = sum32(sum16(sum8(q)));
    const float mb = rbf(mean), sd = rbf(sqrtf(rbf(rbf(q * (1.f / D)) + 1e-5f)));
    const float xn0 = rbf(bflo(xr) - mb) / sd, xn1 = rbf(bfhi(xr) - mb) / sd;
    const v4u a0 = afrag(xn0, msk), a1 = afrag(xn1, msk);
    TS(1);
#pragma unroll
    for (int sg = 0; sg < 3; ++sg) {
      f4 acc = {0.f, 0.f, 0.f, 0.f};
      acc = mfma(a0, wa[0][sg], acc);
      acc = mfma(a1, wa[1][sg], acc);
      *(f4*)&red[w * 768 + sg * 256 + (g4 >> 1) * 128 + 8 * j + 4 * (g4 & 1)] = acc;
    }
  }
  TS(2);
  kb[3] = LDK(3);
  BAR();
  if (hasA && w < 6) {
    float s0 = 0.f, s1 = 0.f;
    if (w < 4) {
#pragma unroll
      for (int ww = 0; ww < 8; ++ww) { const f2 t = *(const f2*)&red[ww * 768 + w * 128 + 2 * lane]; s0 += t.x; s1 += t.y; }
    } else {
#pragma unroll
      for (int ww = 0; ww < 8; ++ww) { s0 += red[ww * 768 + w * 128 + lane]; s1 += red[ww * 768 + w * 128 + 64 + lane]; }
    }
    const int off = w < 4 ? WS_QP + ((ks * 2 + (w & 1)) * 3072 + 128 * cgg + (w >> 1) * 64) * 16 : WS_GU + ((ks * 2 + (w & 1)) * 1536 + 64 * cgg) * 16;
    stws(rW, off + lane * 16, s0, s1, tag);
  }
  TS(3);
#pragma unroll
  for (int i = 4; i < 9; ++i) { PACE(); kb[i] = LDK(i); }
  v4u qg[5];
  const int qoff = WS_QP + (((lane >> 5) * 2 + b) * 3072 + 96 * h + 32 * (w - 5) + (lane & 31)) * 16;
  if (w >= 5) {
#pragma unroll
    for (int i = 0; i < 5; ++i) qg[i] = __builtin_amdgcn_raw_buffer_load_b128(rW, qoff, i * 4 * 3072 * 16, SC1);
  }
  SB();
  kb[9] = LDK(9);
#pragma unroll
  for (int i = 10; i < 13; ++i) { PACE(); kb[i] = LDK(i); }
  SB();
  unsigned* qs = (unsigned*)&red[8 * 768];
  if (w >= 5) {
    AWAIT(bad(qg[0], tag) | bad(qg[1], tag) | bad(qg[2], tag) | bad(qg[3], tag) | bad(qg[4], tag),
          _Pragma("unroll") for (int i = 0; i < 5; ++i) qg[i] = __builtin_amdgcn_raw_buffer_load_b128(rW, qoff, i * 4 * 3072 * 16, SC1));
    float s0 = 0.f, s1 = 0.f;
#pragma unroll
    for (int i = 0; i < 5; ++i) { s0 += asf(qg[i].x); s1 += asf(qg[i].z); }
    s0 = sum32(s0); s1 = sum32(s1);
    if (lane < 32) qs[32 * (w - 5) + lane] = pk(s0, s1);
  }
  TS(4);
  PACE(); kb[13] = LDK(13);
  PACE(); kb[14] = LDK(14);
  BAR();
  TS(5);
  v4u qv[3];
#pragma unroll
  for (int c = 0; c < 3; ++c) qv[c] = *(const v4u*)&qs[32 * c + 4 * e];
  float sc[8];
#define QK(t) do { float d_ = 0.f; _Pragma("unroll") for (int c = 0; c < 3; ++c) _Pragma("unroll") for (int k = 0; k < 4; ++k) d_ = dot2(kb[3 * (t) + c][k], qv[c][k], d_); \
    sc[t] = rbf(rbf(sum8(d_)) * 0.07216878364870323f) * 1.4426950408889634f; } while (0)
  PACE(); kb[15] = LDK(15); SB(); QK(0);
  PACE(); kb[16] = LDK(16); SB(); QK(1);
  PACE(); kb[17] = LDK(17); SB(); QK(2);
  PACE(); kb[18] = LDK(18); SB(); QK(3);
  PACE(); kb[19] = LDK(19);
  PACE(); kb[20] = LDK(20); SB(); QK(4);
  v4u gp0, gp1;
  const int goff = WS_GU + ((e * 2 + lb) * 1536 + 32 * rg + 4 * w + g4) * 16;
  if (hasF) { gp0 = ldws(rW, goff); gp1 = ldws(rW, e < 2 ? goff + 16 * 1536 * 16 : goff); }
  SB();
  kb[21] = LDK(21);
  PACE(); kb[22] = LDK(22);
  PACE(); kb[23] = LDK(23); SB(); QK(5);
  PACE(); vb[0] = LDV(0);
  PACE(); vb[1] = LDV(1);
  PACE(); vb[2] = LDV(2); SB(); QK(6);
  v4u ah;
  if (hasF) {
    AWAIT(bad(gp0, tag) | bad(gp1, tag), gp0 = ldws(rW, goff); gp1 = ldws(rW, e < 2 ? goff + 16 * 1536 * 16 : goff));
    const float g = rbf(sum8(asf(gp0.x) + (e < 2 ? asf(gp1.x) : 0.f))), u = rbf(sum8(asf(gp0.z) + (e < 2 ? asf(gp1.z) : 0.f)));
    const float z = 0.7978845608028654f * (g + 0.044715f * g * g * g);
    const float th = 1.f - 2.f / (1.f + __expf(2.f * z));
    ah = afrag(rbf(0.5f * g * (1.f + th)) * u, msk);
  }
  PACE(); vb[3] = LDV(3);
  PACE(); vb[4] = LDV(4);
  PACE(); vb[5] = LDV(5); SB(); QK(7);
  TS(6);
  float m = fmaxf(fmaxf(fmaxf(sc[0], sc[1]), fmaxf(sc[2], sc[3])), fmaxf(fmaxf(sc[4], sc[5]), fmaxf(sc[6], sc[7])));
  m = max32(max16(fmaxf(m, dpp<R8>(m))));
  float l = 0.f;
#pragma unroll
  for (int t = 0; t < 8; ++t) { sc[t] = __builtin_amdgcn_exp2f(sc[t] - m); l += sc[t]; }
  l = sum32(sum16(l + dpp<R8>(l)));
  const int stoff = WS_ST + (b * NH + h) * 512;
  if (lane == 0) stws(rW, stoff + (sp * 8 + w) * 16, m, l, tag);
  f4 o0 = {0.f, 0.f, 0.f, 0.f}, o1 = o0, o2 = o0;
#define PV(t) do { const v4u ap_ = afrag(sc[t], msk); o0 = mfma(ap_, vb[3 * (t)], o0); o1 = mfma(ap_, vb[3 * (t) + 1], o1); o2 = mfma(ap_, vb[3 * (t) + 2], o2); } while (0)
  SB();
  vb[6] = LDV(6);
  PACE(); vb[7] = LDV(7);
  PACE(); vb[8] = LDV(8); SB(); PV(0);
  PACE(); vb[9] = LDV(9);
  PACE(); vb[10] = LDV(10);
  PACE(); vb[11] = LDV(11); SB(); PV(1);
  PACE(); vb[12] = LDV(12);
  PACE(); vb[13] = LDV(13);
  PACE(); vb[14] = LDV(14); SB(); PV(2);
  v4u stp = ldws(rW, stoff + (lane & 31) * 16);
  SB();
  vb[15] = LDV(15);
  PACE(); vb[16] = LDV(16);
  PACE(); vb[17] = LDV(17); SB(); PV(3);
  PACE(); vb[18] = LDV(18);
  PACE(); vb[19] = LDV(19);
  PACE(); vb[20] = LDV(20); SB(); PV(4);
  AWAIT(bad(stp, tag), stp = ldws(rW, stoff + (lane & 31) * 16));
  float fs;
  {
    float M = asf(stp.x);
    M = fmaxf(M, dpp<X1>(M)); M = fmaxf(M, dpp<X2>(M)); M = fmaxf(M, dpp<HM>(M)); M = max16(fmaxf(M, dpp<R8>(M)));
    float L = asf(stp.z) * __builtin_amdgcn_exp2f(asf(stp.x) - M);
    L = sum8(L); L = sum16(L + dpp<R8>(L));
    fs = __builtin_amdgcn_exp2f(m - M) / L;
  }
  PACE(); vb[21] = LDV(21);
  PACE(); vb[22] = LDV(22);
  PACE(); vb[23] = LDV(23); SB(); PV(5);
  TS(7);
  v4u fw[5];
  if (hasF) {
    const rsrc rO = mkr(wo + (size_t)128 * rg * D + 128 * cg, 128 * D * 2), rD = mkr(wd + (size_t)32 * rg * D + 128 * cg, 32 * D * 2);
    const int fo = (16 * w + 4 * g4) * 1280 + j * 16;
    PACE(); fw[4] = __builtin_amdgcn_raw_buffer_load_b128(rD, (4 * w + g4) * 1280 + j * 16, 0, NT);
    PACE(); fw[0] = __builtin_amdgcn_raw_buffer_load_b128(rO, fo, 0, NT); SB(); PV(6);
    PACE(); fw[1] = __builtin_amdgcn_raw_buffer_load_b128(rO, fo + 1280, 0, NT);
    PACE(); fw[2] = __builtin_amdgcn_raw_buffer_load_b128(rO, fo + 2560, 0, NT);
    SB(); fw[3] = __builtin_amdgcn_raw_buffer_load_b128(rO, fo + 3840, 0, NT); SB();
    PV(7);
  } else {
    PV(6); PV(7);
  }
  TS(8);
  if ((lane >> 5) == lb) {
    float* p = &red[(w * 2 + lb) * 192 + 8 * e + 4 * (g4 & 1)];
    *(f4*)(p) = o0 * fs; *(f4*)(p + 64) = o1 * fs; *(f4*)(p + 128) = o2 * fs;
  }
  BAR();
  if (tid < 96) {
    float s0 = 0.f, s1 = 0.f;
#pragma unroll
    for (int i = 0; i < 16; ++i) { const f2 t = *(const f2*)&red[i * 192 + 2 * tid]; s0 += t.x; s1 += t.y; }
    stws(rW, WS_OP + ((b * 4 + sp) * 3072 + 96 * h + tid) * 16, s0, s1, tag);
  }
  TS(9);
  if (hasF) {
    const int ooff = WS_OP + ((lb * 4 + (e & 3)) * 3072 + 64 * rg + 8 * w + 2 * g4 + (e >> 2)) * 16;
    f4 acc = {0.f, 0.f, 0.f, 0.f};
    acc = mfma(ah, fw[4], acc);
    SB();
    asm volatile("s_waitcnt vmcnt(2)");
    SB();
    v4u op = ldws(rW, ooff);
    AWAIT(bad(op, tag), op = ldws(rW, ooff));
    TS(10);
    float u0 = asf(op.x), u1 = asf(op.z);
    u0 += dpp<X1>(u0); u1 += dpp<X1>(u1); u0 += dpp<X2>(u0); u1 += dpp<X2>(u1);
    const float t0 = dpp<HM>(u0), t1 = dpp<HM>(u1);
    const bool hi = e >= 4;
    acc = mfma(afrag(hi ? t0 : u0, msk), fw[0], acc);
    acc = mfma(afrag(hi ? t1 : u1, msk), fw[1], acc);
    acc = mfma(afrag(hi ? u0 : t0, msk), fw[2], acc);
    acc = mfma(afrag(hi ? u1 : t1, msk), fw[3], acc);
    *(f4*)&red[3072 + w * 256 + (g4 >> 1) * 128 + 8 * j + 4 * (g4 & 1)] = acc;
    BAR();
    if (tid < 128) {
      float s0 = 0.f, s1 = 0.f;
#pragma unroll
      for (int ww = 0; ww < 8; ++ww) { const f2 t = *(const f2*)&red[3072 + ww * 256 + 2 * tid]; s0 += t.x; s1 += t.y; }
      stws(rW, WS_FP + (rg * 640 + (tid >> 6) * 320 + 64 * cg + (tid & 63)) * 16, s0, s1, tag);
    }
    TS(11);
  } else if (w < 5) {
    const int rho = (xg * 2 + (r - 30)) * 5 + w;
    const int foff = WS_FP + (a * 640 + 8 * rho + e) * 16;
    const unsigned xv = __builtin_amdgcn_raw_buffer_load_b32(rX, (8 * rho + e) * 4, 0, 0);
    v4u fp[6];
    unsigned spin = 0;
#pragma nounroll
    do {
      asm volatile("" ::: "memory");
#pragma unroll
      for (int i = 0; i < 6; ++i) fp[i] = __builtin_amdgcn_raw_buffer_load_b128(rW, foff, i * 8 * 640 * 16, SC1);
    } while (__any((bad(fp[0], tag) | bad(fp[1], tag) | bad(fp[2], tag) | bad(fp[3], tag) | bad(fp[4], tag) | bad(fp[5], tag)) != 0u) && ++spin < SPIN_CAP);
    TS(10);
    float s0 = 0.f, s1 = 0.f;
#pragma unroll
    for (int i = 0; i < 6; ++i) { s0 += asf(fp[i].x); s1 += asf(fp[i].z); }
    s0 = sum32(sum16(s0 + dpp<R8>(s0))); s1 = sum32(sum16(s1 + dpp<R8>(s1)));
    if (lane < 8) ((unsigned*)out)[8 * rho + lane] = pk(bflo(xv) + s0, bfhi(xv) + s1);
    if (rho == 0 && lane == 0) __builtin_amdgcn_raw_buffer_store_b32(tag, rW, WS_E, 0, SC1);
    TS(11);
  }
}
}

extern "C" void kernel_launch(void* const* d_in, const int* in_sizes, int n_in, void* d_out, int out_size, void* d_ws, size_t ws_size, hipStream_t stream) {
  fused<<<256, 512, 0, stream>>>((const u16*)d_in[0], (const u16*)d_in[1], (const u16*)d_in[2], (const u16*)d_in[3], (const u16*)d_in[4],
                                 (const u16*)d_in[6], (const u16*)d_in[7], (const u16*)d_in[8], (u16*)d_out, (unsigned char*)d_ws);
}
```

```cpp
#include <hip/hip_runtime.h>
#include <hip/hip_bf16.h>
#include <stdint.h>

namespace {
typedef unsigned v4u __attribute__((ext_vector_type(4)));
typedef unsigned v2u __attribute__((ext_vector_type(2)));
typedef float f4 __attribute__((ext_vector_type(4)));
typedef float f2 __attribute__((ext_vector_type(2)));
typedef __bf16 bf8 __attribute__((ext_vector_type(8)));
typedef __bf16 bf2 __attribute__((ext_vector_type(2)));
typedef unsigned short u16;
#define GAS __attribute__((address_space(1)))

constexpr int D = 640, NH = 32, SK = 8192, DH = 192, WIN = 2048, DF = 1536;
constexpr int NT = 2, SC1 = 16;
#define WND 4
#define STAMP 0
constexpr int W = WND;
constexpr int WS_E = 0;
constexpr int WS_QP = 4096;
constexpr int WS_GU = WS_QP + 10 * 2 * 3072 * 16;
constexpr int WS_ST = WS_GU + 10 * 2 * 1536 * 16;
constexpr int WS_OP = WS_ST + 64 * 32 * 16;
constexpr int WS_FP = WS_OP + 2 * 4 * 3072 * 16;
constexpr int WS_TS = WS_FP + 48 * 640 * 16;
constexpr int WS_END = WS_TS + 256 * 16;
constexpr int WS_STAMP = 4 << 20;
constexpr unsigned SPIN_CAP = 1u << 16;

__device__ __forceinline__ float asf(unsigned u) { return __builtin_bit_cast(float, u); }
__device__ __forceinline__ unsigned asu(float f) { return __builtin_bit_cast(unsigned, f); }
__device__ __forceinline__ float bflo(unsigned u) { return asf(u << 16); }
__device__ __forceinline__ float bfhi(unsigned u) { return asf(u & 0xffff0000u); }
__device__ __forceinline__ unsigned pk(float lo, float hi) { bf2 r = {(__bf16)lo, (__bf16)hi}; return __builtin_bit_cast(unsigned, r); }
__device__ __forceinline__ float rbf(float f) { return (float)(__bf16)f; }
template <int CTRL> __device__ __forceinline__ float dpp(float x) { return asf(__builtin_amdgcn_mov_dpp(asu(x), CTRL, 0xf, 0xf, true)); }
constexpr int X1 = 0xB1, X2 = 0x4E, HM = 0x141, R8 = 0x128;
__device__ __forceinline__ float sum16(float x) { auto s = __builtin_amdgcn_permlane16_swap(asu(x), asu(x), false, false); return asf(s[0]) + asf(s[1]); }
__device__ __forceinline__ float sum32(float x) { auto s = __builtin_amdgcn_permlane32_swap(asu(x), asu(x), false, false); return asf(s[0]) + asf(s[1]); }
__device__ __forceinline__ float max16(float x) { auto s = __builtin_amdgcn_permlane16_swap(asu(x), asu(x), false, false); return fmaxf(asf(s[0]), asf(s[1])); }
__device__ __forceinline__ float max32(float x) { auto s = __builtin_amdgcn_permlane32_swap(asu(x), asu(x), false, false); return fmaxf(asf(s[0]), asf(s[1])); }
__device__ __forceinline__ float sum8(float x) { x += dpp<X1>(x); x += dpp<X2>(x); x += dpp<HM>(x); return x; }
__device__ __forceinline__ float dot2(unsigned a, unsigned b, float c) { return __builtin_amdgcn_fdot2_f32_bf16(__builtin_bit_cast(bf2, a), __builtin_bit_cast(bf2, b), c, false); }
__device__ __forceinline__ f4 mfma(v4u a, v4u b, f4 c) { return __builtin_amdgcn_mfma_f32_16x16x32_bf16(__builtin_bit_cast(bf8, a), __builtin_bit_cast(bf8, b), c, 0, 0, 0); }
__device__ __forceinline__ v4u afrag(float w, v4u m) { const unsigned d = pk(w, w); return v4u{d & m.x, d & m.y, d & m.z, d & m.w}; }
typedef __amdgpu_buffer_rsrc_t rsrc;
__device__ __forceinline__ rsrc mkr(const void* p, int bytes) { return __builtin_amdgcn_make_buffer_rsrc((void*)p, 0, bytes, 0x00020000); }
#define SB() __builtin_amdgcn_sched_barrier(0)
#define PACE() do { SB(); asm volatile("s_waitcnt vmcnt(%0)" :: "n"(W - 1)); SB(); } while (0)
#define BAR() do { SB(); asm volatile("s_waitcnt lgkmcnt(0)\n\ts_barrier" ::: "memory"); SB(); } while (0)
__device__ __forceinline__ v4u ldws(rsrc r, int off) { return __builtin_amdgcn_raw_buffer_load_b128(r, off, 0, SC1); }
__device__ __forceinline__ void stws(rsrc r, int off, float a, float b, unsigned tag) { __builtin_amdgcn_raw_buffer_store_b128(v4u{asu(a), tag, asu(b), tag}, r, off, 0, SC1); }
__device__ __forceinline__ unsigned bad(v4u p, unsigned tag) { return (p.y ^ tag) | (p.w ^ tag); }
#define AWAIT(BAD, RELOAD) do { if (__builtin_expect(__any((BAD) != 0u), 0)) { unsigned spin_ = 0; \
    _Pragma("nounroll") do { asm volatile("" ::: "memory"); RELOAD; } while (__any((BAD) != 0u) && ++spin_ < SPIN_CAP); } } while (0)
#define TS(i)

__global__ void __launch_bounds__(512, 2) fused(const u16* __restrict__ x, const u16* __restrict__ Kc, const u16* __restrict__ Vc, const u16* __restrict__ wq,
                                                const u16* __restrict__ wo, const u16* __restrict__ wg, const u16* __restrict__ wu, const u16* __restrict__ wd,
                                                u16* __restrict__ out, unsigned char* ws) {
  __shared__ __attribute__((aligned(16))) float red[8 * 768 + 128];
  const int tid = threadIdx.x, lane = tid & 63, w = __builtin_amdgcn_readfirstlane(tid >> 6);
  TS(0);
  const unsigned tstart = (unsigned)__builtin_amdgcn_s_memrealtime();
  const int xg = blockIdx.x & 7, r = blockIdx.x >> 3;
  const int h = xg * 4 + (r >> 3), b = (r >> 2) & 1, sp = r & 3;
  const bool hasA = r >= 2, hasF = r < 30;
  const int ua = r - 2, ks = ua % 10, cgg = xg * 3 + ua / 10;
  const int rg = xg * 6 + r / 5, cg = r % 5;
  const int g4 = lane >> 4, j = lane & 15, lb = (lane >> 3) & 1, e = lane & 7, a = lane >> 3;
  const rsrc rW = mkr(ws, WS_END);
  const rsrc rX = mkr(x, 2 * D * 2);
  const size_t kvb = ((size_t)(b * NH + h) * SK + (SK - WIN) + sp * 512) * DH;
  const rsrc rK = mkr(Kc + kvb, 512 * DH * 2), rV = mkr(Vc + kvb, 512 * DH * 2);
  const int kvo = w * 3072 + a * 384 + e * 16;
#define LDK(i) __builtin_amdgcn_raw_buffer_load_b128(rK, kvo + ((i) % 3) * 128, ((i) / 3) * 24576, NT)
#define LDV(i) __builtin_amdgcn_raw_buffer_load_b128(rV, kvo + ((i) % 3) * 128, ((i) / 3) * 24576, NT)

  const unsigned tag = __builtin_amdgcn_raw_buffer_load_b32(rW, WS_E, 0, SC1) + 1u;
  v4u wa[2][3];
  v4u xs0, xs1; v2u xs2; unsigned xr;
  if (hasA) {
    const int jj = e | (g4 << 3);
    xs0 = __builtin_amdgcn_raw_buffer_load_b128(rX, lb * 1280 + jj * 16, 0, 0);
    xs1 = __builtin_amdgcn_raw_buffer_load_b128(rX, lb * 1280 + 512 + jj * 16, 0, 0);
    xs2 = __builtin_amdgcn_raw_buffer_load_b64(rX, lb * 1280 + 1024 + jj * 8, 0, 0);
    const int row0 = 64 * ks + 8 * w + 2 * g4;
    xr = __builtin_amdgcn_raw_buffer_load_b32(rX, lb * 1280 + row0 * 2, 0, 0);
    const rsrc rQ = mkr(wq + 256 * cgg, D * NH * DH * 2 - 512 * cgg);
    const GAS u16* gu = (const GAS u16*)(lb ? wu : wg) + (size_t)row0 * DF + 64 * cgg + 8 * e;
#pragma unroll
    for (int rq = 0; rq < 2; ++rq) {
      wa[rq][0] = __builtin_amdgcn_raw_buffer_load_b128(rQ, row0 * 12288 + j * 16, rq * 12288, NT);
      wa[rq][1] = __builtin_amdgcn_raw_buffer_load_b128(rQ, row0 * 12288 + j * 16 + 256, rq * 12288, NT);
      wa[rq][2] = __builtin_nontemporal_load((const GAS v4u*)(gu + rq * DF));
    }
  }
  SB();
  v4u kb[24], vb[24];
  kb[0] = LDK(0); kb[1] = LDK(1); kb[2] = LDK(2);
  SB();
  const v4u msk = {(e >> 1) == 0 ? ((e & 1) ? 0xffff0000u : 0xffffu) : 0u, (e >> 1) == 1 ? ((e & 1) ? 0xffff0000u : 0xffffu) : 0u,
                   (e >> 1) == 2 ? ((e & 1) ? 0xffff0000u : 0xffffu) : 0u, (e >> 1) == 3 ? ((e & 1) ? 0xffff0000u : 0xffffu) : 0u};
  if (hasA) {
    float v[20];
#pragma unroll
    for (int i = 0; i < 4; ++i) { v[2 * i] = bflo(xs0[i]); v[2 * i + 1] = bfhi(xs0[i]); v[8 + 2 * i] = bflo(xs1[i]); v[9 + 2 * i] = bfhi(xs1[i]); }
    v[16] = bflo(xs2[0]); v[17] = bfhi(xs2[0]); v[18] = bflo(xs2[1]); v[19] = bfhi(xs2[1]);
    float s = 0.f;
#pragma unroll
    for (int i = 0; i < 20; ++i) s += v[i];
    s = sum32(sum16(sum8(s)));
    const float mean = s * (1.f / D);
    float q = 0.f;
#pragma unroll
    for (int i = 0; i < 20; ++i) { const float d = v[i] - mean; q = fmaf(d, d, q); }
    q = sum32(sum16(sum8(q)));
    const float mb = rbf(mean), sd = rbf(sqrtf(rbf(rbf(q * (1.f / D)) + 1e-5f)));
    const float xn0 = rbf(bflo(xr) - mb) / sd, xn1 = rbf(bfhi(xr) - mb) / sd;
    const v4u a0 = afrag(xn0, msk), a1 = afrag(xn1, msk);
    TS(1);
#pragma unroll
    for (int sg = 0; sg < 3; ++sg) {
      f4 acc = {0.f, 0.f, 0.f, 0.f};
      acc = mfma(a0, wa[0][sg], acc);
      acc = mfma(a1, wa[1][sg], acc);
      *(f4*)&red[w * 768 + sg * 256 + (g4 >> 1) * 128 + 8 * j + 4 * (g4 & 1)] = acc;
    }
  }
  TS(2);
  kb[3] = LDK(3);
  BAR();
  if (hasA && w < 6) {
    float s0 = 0.f, s1 = 0.f;
    if (w < 4) {
#pragma unroll
      for (int ww = 0; ww < 8; ++ww) { const f2 t = *(const f2*)&red[ww * 768 + w * 128 + 2 * lane]; s0 += t.x; s1 += t.y; }
    } else {
#pragma unroll
      for (int ww = 0; ww < 8; ++ww) { s0 += red[ww * 768 + w * 128 + lane]; s1 += red[ww * 768 + w * 128 + 64 + lane]; }
    }
    const int off = w < 4 ? WS_QP + ((ks * 2 + (w & 1)) * 3072 + 128 * cgg + (w >> 1) * 64) * 16 : WS_GU + ((ks * 2 + (w & 1)) * 1536 + 64 * cgg) * 16;
    stws(rW, off + lane * 16, s0, s1, tag);
  }
  if (tid == 0) stws(rW, WS_TS + blockIdx.x * 16, asf(tstart), 0.f, tag);
  TS(3);
#pragma unroll
  for (int i = 4; i < 9; ++i) { PACE(); kb[i] = LDK(i); }
  v4u qg[5];
  const int qoff = WS_QP + (((lane >> 5) * 2 + b) * 3072 + 96 * h + 32 * (w - 5) + (lane & 31)) * 16;
  if (w >= 5) {
#pragma unroll
    for (int i = 0; i < 5; ++i) qg[i] = __builtin_amdgcn_raw_buffer_load_b128(rW, qoff, i * 4 * 3072 * 16, SC1);
  }
  SB();
  kb[9] = LDK(9);
#pragma unroll
  for (int i = 10; i < 13; ++i) { PACE(); kb[i] = LDK(i); }
  SB();
  unsigned* qs = (unsigned*)&red[8 * 768];
  if (w >= 5) {
    AWAIT(bad(qg[0], tag) | bad(qg[1], tag) | bad(qg[2], tag) | bad(qg[3], tag) | bad(qg[4], tag),
          _Pragma("unroll") for (int i = 0; i < 5; ++i) qg[i] = __builtin_amdgcn_raw_buffer_load_b128(rW, qoff, i * 4 * 3072 * 16, SC1));
    float s0 = 0.f, s1 = 0.f;
#pragma unroll
    for (int i = 0; i < 5; ++i) { s0 += asf(qg[i].x); s1 += asf(qg[i].z); }
    s0 = sum32(s0); s1 = sum32(s1);
    if (lane < 32) qs[32 * (w - 5) + lane] = pk(s0, s1);
  }
  TS(4);
  PACE(); kb[13] = LDK(13);
  PACE(); kb[14] = LDK(14);
  BAR();
  TS(5);
  v4u qv[3];
#pragma unroll
  for (int c = 0; c < 3; ++c) qv[c] = *(const v4u*)&qs[32 * c + 4 * e];
  float sc[8];
#define QK(t) do { float d_ = 0.f; _Pragma("unroll") for (int c = 0; c < 3; ++c) _Pragma("unroll") for (int k = 0; k < 4; ++k) d_ = dot2(kb[3 * (t) + c][k], qv[c][k], d_); \
    sc[t] = rbf(rbf(sum8(d_)) * 0.07216878364870323f) * 1.4426950408889634f; } while (0)
  PACE(); kb[15] = LDK(15); SB(); QK(0);
  PACE(); kb[16] = LDK(16); SB(); QK(1);
  PACE(); kb[17] = LDK(17); SB(); QK(2);
  PACE(); kb[18] = LDK(18); SB(); QK(3);
  PACE(); kb[19] = LDK(19);
  PACE(); kb[20] = LDK(20); SB(); QK(4);
  v4u gp0, gp1;
  const int goff = WS_GU + ((e * 2 + lb) * 1536 + 32 * rg + 4 * w + g4) * 16;
  if (hasF) { gp0 = ldws(rW, goff); gp1 = ldws(rW, e < 2 ? goff + 16 * 1536 * 16 : goff); }
  SB();
  kb[21] = LDK(21);
  PACE(); kb[22] = LDK(22);
  PACE(); kb[23] = LDK(23); SB(); QK(5);
  PACE(); vb[0] = LDV(0);
  PACE(); vb[1] = LDV(1);
  PACE(); vb[2] = LDV(2); SB(); QK(6);
  v4u ah;
  if (hasF) {
    AWAIT(bad(gp0, tag) | bad(gp1, tag), gp0 = ldws(rW, goff); gp1 = ldws(rW, e < 2 ? goff + 16 * 1536 * 16 : goff));
    const float g = rbf(sum8(asf(gp0.x) + (e < 2 ? asf(gp1.x) : 0.f))), u = rbf(sum8(asf(gp0.z) + (e < 2 ? asf(gp1.z) : 0.f)));
    const float z = 0.7978845608028654f * (g + 0.044715f * g * g * g);
    const float th = 1.f - 2.f / (1.f + __expf(2.f * z));
    ah = afrag(rbf(0.5f * g * (1.f + th)) * u, msk);
  }
  PACE(); vb[3] = LDV(3);
  PACE(); vb[4] = LDV(4);
  PACE(); vb[5] = LDV(5); SB(); QK(7);
  TS(6);
  float m = fmaxf(fmaxf(fmaxf(sc[0], sc[1]), fmaxf(sc[2], sc[3])), fmaxf(fmaxf(sc[4], sc[5]), fmaxf(sc[6], sc[7])));
  m = max32(max16(fmaxf(m, dpp<R8>(m))));
  float l = 0.f;
#pragma unroll
  for (int t = 0; t < 8; ++t) { sc[t] = __builtin_amdgcn_exp2f(sc[t] - m); l += sc[t]; }
  l = sum32(sum16(l + dpp<R8>(l)));
  const int stoff = WS_ST + (b * NH + h) * 512;
  if (lane == 0) stws(rW, stoff + (sp * 8 + w) * 16, m, l, tag);
  f4 o0 = {0.f, 0.f, 0.f, 0.f}, o1 = o0, o2 = o0;
#define PV(t) do { const v4u ap_ = afrag(sc[t], msk); o0 = mfma(ap_, vb[3 * (t)], o0); o1 = mfma(ap_, vb[3 * (t) + 1], o1); o2 = mfma(ap_, vb[3 * (t) + 2], o2); } while (0)
  SB();
  vb[6] = LDV(6);
  PACE(); vb[7] = LDV(7);
  PACE(); vb[8] = LDV(8); SB(); PV(0);
  PACE(); vb[9] = LDV(9);
  PACE(); vb[10] = LDV(10);
  PACE(); vb[11] = LDV(11); SB(); PV(1);
  PACE(); vb[12] = LDV(12);
  PACE(); vb[13] = LDV(13);
  PACE(); vb[14] = LDV(14); SB(); PV(2);
  v4u stp = ldws(rW, stoff + (lane & 31) * 16);
  SB();
  vb[15] = LDV(15);
  PACE(); vb[16] = LDV(16);
  PACE(); vb[17] = LDV(17); SB(); PV(3);
  PACE(); vb[18] = LDV(18);
  PACE(); vb[19] = LDV(19);
  PACE(); vb[20] = LDV(20); SB(); PV(4);
  AWAIT(bad(stp, tag), stp = ldws(rW, stoff + (lane & 31) * 16));
  float fs;
  {
    float M = asf(stp.x);
    M = fmaxf(M, dpp<X1>(M)); M = fmaxf(M, dpp<X2>(M)); M = fmaxf(M, dpp<HM>(M)); M = max16(fmaxf(M, dpp<R8>(M)));
    float L = asf(stp.z) * __builtin_amdgcn_exp2f(asf(stp.x) - M);
    L = sum8(L); L = sum16(L + dpp<R8>(L));
    fs = __builtin_amdgcn_exp2f(m - M) / L;
  }
  PACE(); vb[21] = LDV(21);
  PACE(); vb[22] = LDV(22);
  PACE(); vb[23] = LDV(23); SB(); PV(5);
  TS(7);
  v4u fw[5];
  if (hasF) {
    const rsrc rO = mkr(wo + (size_t)128 * rg * D + 128 * cg, 128 * D * 2), rD = mkr(wd + (size_t)32 * rg * D + 128 * cg, 32 * D * 2);
    const int fo = (16 * w + 4 * g4) * 1280 + j * 16;
    PACE(); fw[4] = __builtin_amdgcn_raw_buffer_load_b128(rD, (4 * w + g4) * 1280 + j * 16, 0, NT);
    PACE(); fw[0] = __builtin_amdgcn_raw_buffer_load_b128(rO, fo, 0, NT); SB(); PV(6);
    PACE(); fw[1] = __builtin_amdgcn_raw_buffer_load_b128(rO, fo + 1280, 0, NT);
    PACE(); fw[2] = __builtin_amdgcn_raw_buffer_load_b128(rO, fo + 2560, 0, NT);
    SB(); fw[3] = __builtin_amdgcn_raw_buffer_load_b128(rO, fo + 3840, 0, NT); SB();
    PV(7);
  } else {
    PV(6); PV(7);
  }
  TS(8);
  if ((lane >> 5) == lb) {
    float* p = &red[(w * 2 + lb) * 192 + 8 * e + 4 * (g4 & 1)];
    *(f4*)(p) = o0 * fs; *(f4*)(p + 64) = o1 * fs; *(f4*)(p + 128) = o2 * fs;
  }
  BAR();
  if (tid < 96) {
    float s0 = 0.f, s1 = 0.f;
#pragma unroll
    for (int i = 0; i < 16; ++i) { const f2 t = *(const f2*)&red[i * 192 + 2 * tid]; s0 += t.x; s1 += t.y; }
    stws(rW, WS_OP + ((b * 4 + sp) * 3072 + 96 * h + tid) * 16, s0, s1, tag);
  }
  TS(9);
  if (hasF) {
    const int ooff = WS_OP + ((lb * 4 + (e & 3)) * 3072 + 64 * rg + 8 * w + 2 * g4 + (e >> 2)) * 16;
    f4 acc = {0.f, 0.f, 0.f, 0.f};
    acc = mfma(ah, fw[4], acc);
    SB();
    asm volatile("s_waitcnt vmcnt(2)");
    SB();
    v4u op = ldws(rW, ooff);
    AWAIT(bad(op, tag), op = ldws(rW, ooff));
    TS(10);
    float u0 = asf(op.x), u1 = asf(op.z);
    u0 += dpp<X1>(u0); u1 += dpp<X1>(u1); u0 += dpp<X2>(u0); u1 += dpp<X2>(u1);
    const float t0 = dpp<HM>(u0), t1 = dpp<HM>(u1);
    const bool hi = e >= 4;
    acc = mfma(afrag(hi ? t0 : u0, msk), fw[0], acc);
    acc = mfma(afrag(hi ? t1 : u1, msk), fw[1], acc);
    acc = mfma(afrag(hi ? u0 : t0, msk), fw[2], acc);
    acc = mfma(afrag(hi ? u1 : t1, msk), fw[3], acc);
    *(f4*)&red[3072 + w * 256 + (g4 >> 1) * 128 + 8 * j + 4 * (g4 & 1)] = acc;
    BAR();
    if (tid < 128) {
      float s0 = 0.f, s1 = 0.f;
#pragma unroll
      for (int ww = 0; ww < 8; ++ww) { const f2 t = *(const f2*)&red[3072 + ww * 256 + 2 * tid]; s0 += t.x; s1 += t.y; }
      stws(rW, WS_FP + (rg * 640 + (tid >> 6) * 320 + 64 * cg + (tid & 63)) * 16, s0, s1, tag);
    }
    TS(11);
  } else if (w < 5) {
    const int rho = (xg * 2 + (r - 30)) * 5 + w;
    const int foff = WS_FP + (a * 640 + 8 * rho + e) * 16;
    const unsigned xv = __builtin_amdgcn_raw_buffer_load_b32(rX, (8 * rho + e) * 4, 0, 0);
    v4u fp[6];
    unsigned spin = 0;
#pragma nounroll
    do {
      asm volatile("" ::: "memory");
#pragma unroll
      for (int i = 0; i < 6; ++i) fp[i] = __builtin_amdgcn_raw_buffer_load_b128(rW, foff, i * 8 * 640 * 16, SC1);
    } while (__any((bad(fp[0], tag) | bad(fp[1], tag) | bad(fp[2], tag) | bad(fp[3], tag) | bad(fp[4], tag) | bad(fp[5], tag)) != 0u) && ++spin < SPIN_CAP);
    TS(10);
    float s0 = 0.f, s1 = 0.f;
#pragma unroll
    for (int i = 0; i < 6; ++i) { s0 += asf(fp[i].x); s1 += asf(fp[i].z); }
    s0 = sum32(sum16(s0 + dpp<R8>(s0))); s1 = sum32(sum16(s1 + dpp<R8>(s1)));
    if (lane < 8) ((unsigned*)out)[8 * rho + lane] = pk(bflo(xv) + s0, bfhi(xv) + s1);
    if (rho == 0 && lane == 0) __builtin_amdgcn_raw_buffer_store_b32(tag, rW, WS_E, 0, SC1);
    TS(11);
  }
  if (blockIdx.x == 0 && w == 7) {
    v4u p[4];
    for (int i = 0; i < 4; ++i) p[i] = ldws(rW, WS_TS + (i * 64 + lane) * 16);
    AWAIT(bad(p[0], tag) | bad(p[1], tag) | bad(p[2], tag) | bad(p[3], tag), for (int i = 0; i < 4; ++i) p[i] = ldws(rW, WS_TS + (i * 64 + lane) * 16));
    int d = min(min((int)(p[0].x - tstart), (int)(p[1].x - tstart)), min((int)(p[2].x - tstart), (int)(p[3].x - tstart)));
    for (int o = 32; o; o >>= 1) d = min(d, __shfl_xor(d, o));
    const unsigned base = tstart + __builtin_amdgcn_readfirstlane(d);
    while ((unsigned)__builtin_amdgcn_s_memrealtime() - base < 6000u) __builtin_amdgcn_s_sleep(1);
  }
}
}

extern "C" void kernel_launch(void* const* d_in, const int* in_sizes, int n_in, void* d_out, int out_size, void* d_ws, size_t ws_size, hipStream_t stream) {
  fused<<<256, 512, 0, stream>>>((const u16*)d_in[0], (const u16*)d_in[1], (const u16*)d_in[2], (const u16*)d_in[3], (const u16*)d_in[4],
                                 (const u16*)d_in[6], (const u16*)d_in[7], (const u16*)d_in[8], (u16*)d_out, (unsigned char*)d_ws);
}
```

```cpp
#include <hip/hip_runtime.h>
#include <hip/hip_bf16.h>
#include <stdint.h>

namespace {
typedef unsigned v4u __attribute__((ext_vector_type(4)));
typedef unsigned v2u __attribute__((ext_vector_type(2)));
typedef float f4 __attribute__((ext_vector_type(4)));
typedef float f2 __attribute__((ext_vector_type(2)));
typedef __bf16 bf8 __attribute__((ext_vector_type(8)));
typedef __bf16 bf2 __attribute__((ext_vector_type(2)));
typedef unsigned short u16;
#define GAS __attribute__((address_space(1)))
#define LAS __attribute__((address_space(3)))

constexpr int D = 640, NH = 32, SK = 8192, DH = 192, WIN = 2048, DF = 1536;
constexpr int NT = 2, SC1 = 16;
#define WND 4
#define STAMP 0
#define TAU 0
#define TOFF 0
#define ABL 0
#define FPOS 24
#define CAD 0
#define DEP 0
#define CAD3 0
#define DEP3 0
#define PDELAY 8
#define STSLEEP 16
#define QT 500
#define AW 3
#define QCHK 20
#define RMAP 0
#define PERM 1
#define PERM2 1
#define LSEFULL 1
#define GUW 1
#define CODELEN 11264
#define ALIGNK 4096
#define PAD1 256
#define PAD2 0
#define PAD3 1024
#define QSLEEP 30
#define CODEPF 0
#define KPRE 1
constexpr int W = WND;
constexpr int WS_E = 0;
constexpr int QS = 1536 * 16 + 768, GS = 384 * 16 + 128, OR = 896, OS = 32 * OR + 512, FS = 320 * 16 + 128;
constexpr int WS_QP = 4096;
constexpr int WS_GU = WS_QP + 10 * QS;
constexpr int WS_OP = WS_GU + 20 * GS;
constexpr int WS_FP = WS_OP + 8 * OS;
constexpr int WS_END = WS_FP + 48 * FS;
static_assert(WS_GU % 128 == 0 && WS_OP % 128 == 0 && WS_FP % 128 == 0, "line aligned");
constexpr int WS_STAMP = 4 << 20;
constexpr unsigned SPIN_CAP = 1u << 16;
constexpr int L_PART = 0, PST = 196;
constexpr int L_FIN = 16 * PST;
constexpr int L_Q = L_FIN + 2048;
constexpr int L_H = L_Q + 96;
constexpr int L_O = L_H + 64;
constexpr int L_ST = L_O + 256;
constexpr int L_FW = L_ST + 16;
constexpr int L_FLAG = L_FW + 8;
constexpr int L_XN = L_FLAG + 8;
constexpr int L_END = L_XN + 1280;

__device__ __forceinline__ float asf(unsigned u) { return __builtin_bit_cast(float, u); }
__device__ __forceinline__ unsigned asu(float f) { return __builtin_bit_cast(unsigned, f); }
__device__ __forceinline__ float bflo(unsigned u) { return asf(u << 16); }
__device__ __forceinline__ float bfhi(unsigned u) { return asf(u & 0xffff0000u); }
__device__ __forceinline__ unsigned pk(float lo, float hi) { bf2 r = {(__bf16)lo, (__bf16)hi}; return __builtin_bit_cast(unsigned, r); }
__device__ __forceinline__ float rbf(float f) { return (float)(__bf16)f; }
template <int CTRL> __device__ __forceinline__ float dpp(float x) { return asf(__builtin_amdgcn_mov_dpp(asu(x), CTRL, 0xf, 0xf, true)); }
constexpr int X1 = 0xB1, X2 = 0x4E, HM = 0x141, R4 = 0x124, R8 = 0x128;
__device__ __forceinline__ float sum16(float x) { auto s = __builtin_amdgcn_permlane16_swap(asu(x), asu(x), false, false); return asf(s[0]) + asf(s[1]); }
__device__ __forceinline__ float sum32(float x) { auto s = __builtin_amdgcn_permlane32_swap(asu(x), asu(x), false, false); return asf(s[0]) + asf(s[1]); }
__device__ __forceinline__ float max16(float x) { auto s = __builtin_amdgcn_permlane16_swap(asu(x), asu(x), false, false); return fmaxf(asf(s[0]), asf(s[1])); }
__device__ __forceinline__ float max32(float x) { auto s = __builtin_amdgcn_permlane32_swap(asu(x), asu(x), false, false); return fmaxf(asf(s[0]), asf(s[1])); }
__device__ __forceinline__ float sum4(float x) { x += dpp<X1>(x); x += dpp<X2>(x); return x; }
__device__ __forceinline__ float sum8(float x) { x = sum4(x); x += dpp<HM>(x); return x; }
__device__ __forceinline__ float max8(float x) { x = fmaxf(x, dpp<X1>(x)); x = fmaxf(x, dpp<X2>(x)); x = fmaxf(x, dpp<HM>(x)); return x; }
__device__ __forceinline__ float dot2(unsigned a, unsigned b, float c) { return __builtin_amdgcn_fdot2_f32_bf16(__builtin_bit_cast(bf2, a), __builtin_bit_cast(bf2, b), c, false); }
__device__ __forceinline__ f4 mfma(v4u a, v4u b, f4 c) { return __builtin_amdgcn_mfma_f32_16x16x32_bf16(__builtin_bit_cast(bf8, a), __builtin_bit_cast(bf8, b), c, 0, 0, 0); }
__device__ __forceinline__ v4u afrag(float w, v4u m) { const unsigned d = pk(w, w); return v4u{d & m.x, d & m.y, d & m.z, d & m.w}; }
typedef __amdgpu_buffer_rsrc_t rsrc;
__device__ __forceinline__ rsrc mkr(const void* p, int bytes) { return __builtin_amdgcn_make_buffer_rsrc((void*)p, 0, bytes, 0x00020000); }
#define SB() __builtin_amdgcn_sched_barrier(0)
#define MEMF() asm volatile("" ::: "memory")
#define BAR() do { SB(); asm volatile("s_waitcnt lgkmcnt(0)\n\ts_barrier" ::: "memory"); SB(); } while (0)
__device__ __forceinline__ v4u ldws(rsrc r, int off, int soff = 0) { return __builtin_amdgcn_raw_buffer_load_b128(r, off, soff, SC1); }
__device__ __forceinline__ void st4(rsrc r, int off, f4 v, unsigned tag) {
  const v4u p = {__builtin_amdgcn_perm(asu(v.x), asu(v.y), 0x07060503u), __builtin_amdgcn_perm(asu(v.y), tag, 0x06050100u),
                 __builtin_amdgcn_perm(asu(v.z), asu(v.w), 0x07060503u), __builtin_amdgcn_perm(asu(v.w), tag, 0x06050100u)};
  __builtin_amdgcn_raw_buffer_store_b128(p, r, off, 0, SC1);
}
__device__ __forceinline__ f4 un4(v4u p) {
  return f4{asf(p.x & 0xffffff00u), asf(__builtin_amdgcn_perm(p.x, p.y, 0x0403020cu)), asf(p.z & 0xffffff00u), asf(__builtin_amdgcn_perm(p.z, p.w, 0x0403020cu))};
}
__device__ __forceinline__ unsigned bad4(v4u p, unsigned tag2) { return __builtin_amdgcn_perm(p.y, p.w, 0x05040100u) ^ tag2; }
__device__ __forceinline__ void lds_wait(volatile LAS unsigned* f, unsigned want) {
  for (unsigned s = 0; *f < want && s < (SPIN_CAP << 4); ++s) __builtin_amdgcn_s_sleep(2);
  MEMF();
}
__device__ __forceinline__ void lds_post(LAS unsigned* f) { MEMF(); __hip_atomic_fetch_add(f, 1u, __ATOMIC_RELAXED, __HIP_MEMORY_SCOPE_WORKGROUP); }
__device__ __forceinline__ void sweep5(rsrc r, int off, int soff, int s1, int s2, int s3, unsigned tag2, unsigned epoch, v4u& p0, v4u& p1, v4u& p2, v4u& p3, v2u& st) {
  unsigned t0, t1, t2, cnt;
  asm volatile(
      "s_mov_b32 %[cnt], %[cap]\n\t"
      "s_branch 2f\n"
      "1:\n\t"
      "s_sleep %[cad]\n\t"
      "s_sub_u32 %[cnt], %[cnt], 1\n\t"
      "s_cbranch_scc1 3f\n"
      "2:\n\t"
      "buffer_load_dwordx2 v[80:81], %[soff], %[r], 0 offen sc1\n\t"
      "buffer_load_dwordx4 v[64:67], %[off], %[r], 0 offen sc1\n\t"
      "buffer_load_dwordx4 v[68:71], %[off], %[r], %[s1] offen sc1\n\t"
      "buffer_load_dwordx4 v[72:75], %[off], %[r], %[s2] offen sc1\n\t"
      "buffer_load_dwordx4 v[76:79], %[off], %[r], %[s3] offen sc1\n\t"
      "s_waitcnt vmcnt(2)\n\t"
      "v_xor_b32 %[t2], v81, %[ep]\n\tv_perm_b32 %[t0], v65, v67, %[sel]\n\tv_perm_b32 %[t1], v69, v71, %[sel]\n\t"
      "v_xor_b32 %[t0], %[t0], %[tag]\n\tv_xor_b32 %[t1], %[t1], %[tag]\n\tv_or3_b32 %[t0], %[t0], %[t1], %[t2]\n\t"
      "s_waitcnt vmcnt(0)\n\t"
      "v_perm_b32 %[t1], v73, v75, %[sel]\n\tv_perm_b32 %[t2], v77, v79, %[sel]\n\t"
      "v_xor_b32 %[t1], %[t1], %[tag]\n\tv_xor_b32 %[t2], %[t2], %[tag]\n\tv_or3_b32 %[t0], %[t0], %[t1], %[t2]\n\t"
      "v_cmp_ne_u32 vcc, 0, %[t0]\n\ts_cbranch_vccnz 1b\n"
      "3:"
      : "=&{v[64:67]}"(p0), "=&{v[68:71]}"(p1), "=&{v[72:75]}"(p2), "=&{v[76:79]}"(p3), "=&{v[80:81]}"(st), [t0] "=&v"(t0), [t1] "=&v"(t1), [t2] "=&v"(t2), [cnt] "=&s"(cnt)
      : [off] "v"(off), [soff] "v"(soff), [r] "s"(r), [s1] "s"(s1), [s2] "s"(s2), [s3] "s"(s3), [tag] "v"(tag2), [ep] "v"(epoch), [sel] "s"(0x05040100u), [cap] "n"(SPIN_CAP * 4), [cad] "n"(CAD)
      : "vcc", "scc", "memory");
}
__device__ __forceinline__ void sweep3(rsrc r, int off, int s1, int s2, unsigned tag2, v4u& p0, v4u& p1, v4u& p2) {
  unsigned t0, t1, t2, cnt;
  asm volatile(
      "s_mov_b32 %[cnt], %[cap]\n\t"
      "s_branch 2f\n"
      "1:\n\t"
      "s_sleep %[cad]\n\t"
      "s_sub_u32 %[cnt], %[cnt], 1\n\t"
      "s_cbranch_scc1 3f\n"
      "2:\n\t"
      "buffer_load_dwordx4 v[64:67], %[off], %[r], 0 offen sc1\n\t"
      "buffer_load_dwordx4 v[68:71], %[off], %[r], %[s1] offen sc1\n\t"
      "buffer_load_dwordx4 v[72:75], %[off], %[r], %[s2] offen sc1\n\t"
      "s_waitcnt vmcnt(0)\n\t"
      "v_perm_b32 %[t0], v65, v67, %[sel]\n\tv_perm_b32 %[t1], v69, v71, %[sel]\n\tv_perm_b32 %[t2], v73, v75, %[sel]\n\t"
      "v_xor_b32 %[t0], %[t0], %[tag]\n\tv_xor_b32 %[t1], %[t1], %[tag]\n\tv_xor_b32 %[t2], %[t2], %[tag]\n\t"
      "v_or3_b32 %[t0], %[t0], %[t1], %[t2]\n\tv_cmp_ne_u32 vcc, 0, %[t0]\n\ts_cbranch_vccnz 1b\n"
      "3:"
      : "=&{v[64:67]}"(p0), "=&{v[68:71]}"(p1), "=&{v[72:75]}"(p2), [t0] "=&v"(t0), [t1] "=&v"(t1), [t2] "=&v"(t2), [cnt] "=&s"(cnt)
      : [off] "v"(off), [r] "s"(r), [s1] "s"(s1), [s2] "s"(s2), [tag] "v"(tag2), [sel] "s"(0x05040100u), [cap] "n"(SPIN_CAP * 4), [cad] "n"(CAD3)
      : "vcc", "scc", "memory");
}
#define TS(i)
#define FINISH()
#define QUIT() asm volatile("s_endpgm" ::: "memory")
#define PADTO(k) do { if (PAD##k) asm volatile("s_branch 1f\n\t.fill %0, 4, 0xbf800000\n1:" :: "n"(PAD##k / 4)); } while (0)
#define PACE(n) do { SB(); asm volatile("s_waitcnt vmcnt(%0)" :: "n"(W - 1)); SB(); } while (0)

__global__ void __launch_bounds__(768, 3) __attribute__((aligned(ALIGNK))) fused(const u16* __restrict__ x, const u16* __restrict__ Kc, const u16* __restrict__ Vc, const u16* __restrict__ wq,
                                                const u16* __restrict__ wo, const u16* __restrict__ wg, const u16* __restrict__ wu, const u16* __restrict__ wd,
                                                u16* __restrict__ out, unsigned char* ws) {
  __shared__ __attribute__((aligned(16))) float lds_[L_END];
  LAS float* const L = (LAS float*)lds_;
  LAS unsigned* const LU = (LAS unsigned*)lds_;
  const int tid = threadIdx.x, lane = tid & 63, wv = __builtin_amdgcn_readfirstlane(tid >> 6);
  const unsigned tstart = (unsigned)__builtin_amdgcn_s_memrealtime();
  TS(0);
  unsigned long long pc0;
  asm volatile("s_getpc_b64 %0" : "=s"(pc0));
  if (tid < 8) LU[L_FLAG + tid] = 0u;
  const int xg = blockIdx.x & 7, r = blockIdx.x >> 3;
  const int h = xg * 4 + (r >> 3), b = (r >> 2) & 1, sp = r & 3;
  const bool hasA = r >= 2, hasF = r < 30;
  const int rg = xg * 6 + r / 5, cg = r % 5;
  const int g4 = lane >> 4, j = lane & 15, lb = (lane >> 3) & 1, e = lane & 7, a = lane >> 3;
  asm volatile("" :: "s"(x), "s"(Kc), "s"(Vc), "s"(wq), "s"(wo), "s"(wg), "s"(wu), "s"(wd), "s"(out), "s"(ws));
  const rsrc rW = mkr(ws, WS_END);
  const rsrc rX = mkr(x, 2 * D * 2);
  const v4u msk = {(e >> 1) == 0 ? ((e & 1) ? 0xffff0000u : 0xffffu) : 0u, (e >> 1) == 1 ? ((e & 1) ? 0xffff0000u : 0xffffu) : 0u,
                   (e >> 1) == 2 ? ((e & 1) ? 0xffff0000u : 0xffffu) : 0u, (e >> 1) == 3 ? ((e & 1) ? 0xffff0000u : 0xffffu) : 0u};

  if (wv < 3) {
    const int sv = wv;
    unsigned epoch = __builtin_amdgcn_raw_buffer_load_b32(rW, WS_E, 0, SC1);
#define EPOCH() do { asm volatile("" : "+v"(epoch)); epoch += 1u; } while (0)
    const int ua = hasA ? r - 2 : 0, ks = ua % 5, cp = ua / 5;
    const int jj = e | (g4 << 3);
    const v4u xs0 = __builtin_amdgcn_raw_buffer_load_b128(rX, lb * 1280 + jj * 16, 0, 0);
    const v4u xs1 = __builtin_amdgcn_raw_buffer_load_b128(rX, lb * 1280 + 512 + jj * 16, 0, 0);
    const v2u xs2 = __builtin_amdgcn_raw_buffer_load_b64(rX, lb * 1280 + 1024 + jj * 8, 0, 0);
    const int ldb = cp < 4 ? NH * DH * 2 : DF * 2;
    const rsrc rA = mkr(cp < 4 ? wq + 192 * (4 * xg + cp) : (cp == 4 ? wg : wu) + 192 * xg, ((ABL & 1) || !hasA) ? 0 : (D - 1) * ldb + 384);
    const int row0 = 128 * ks + 16 * a;
    v4u wa[16];
#pragma unroll
    for (int t = 0; t < 16; ++t) wa[t] = __builtin_amdgcn_raw_buffer_load_b128(rA, row0 * ldb + 128 * sv + 16 * e, t * ldb, NT);
    BAR();
    EPOCH();
    float v[20];
#pragma unroll
    for (int i = 0; i < 4; ++i) { v[2 * i] = bflo(xs0[i]); v[2 * i + 1] = bfhi(xs0[i]); v[8 + 2 * i] = bflo(xs1[i]); v[9 + 2 * i] = bfhi(xs1[i]); }
    v[16] = bflo(xs2[0]); v[17] = bfhi(xs2[0]); v[18] = bflo(xs2[1]); v[19] = bfhi(xs2[1]);
    float s = 0.f;
#pragma unroll
    for (int i = 0; i < 20; ++i) s += v[i];
    s = sum32(sum16(sum8(s)));
    const float mean = s * (1.f / D);
    float q = 0.f;
#pragma unroll
    for (int i = 0; i < 20; ++i) { const float d = v[i] - mean; q = fmaf(d, d, q); }
    q = sum32(sum16(sum8(q)));
    const float mb = rbf(mean), rs = 1.f / rbf(sqrtf(rbf(rbf(q * (1.f / D)) + 1e-5f)));
#pragma unroll
    for (int i = 0; i < 5; ++i) {
      v4u o;
#pragma unroll
      for (int k = 0; k < 4; ++k) { const float t = rbf(v[4 * i + k] - mb) * rs; o[k] = pk(t, t); }
      *(LAS v4u*)&LU[L_XN + lb * 640 + (i < 4 ? (i >> 1) * 256 + 8 * jj + 4 * (i & 1) : 512 + 4 * jj)] = o;
    }
    MEMF();
    TS(1);
    f4 a0 = {0.f, 0.f, 0.f, 0.f}, a1 = a0;
#pragma unroll
    for (int t4 = 0; t4 < 4; ++t4) {
      const v4u d0 = *(LAS v4u*)&LU[L_XN + row0 + 4 * t4], d1 = *(LAS v4u*)&LU[L_XN + 640 + row0 + 4 * t4];
#pragma unroll
      for (int k = 0; k < 4; ++k) {
        a0 = mfma(v4u{d0[k] & msk.x, d0[k] & msk.y, d0[k] & msk.z, d0[k] & msk.w}, wa[4 * t4 + k], a0);
        a1 = mfma(v4u{d1[k] & msk.x, d1[k] & msk.y, d1[k] & msk.z, d1[k] & msk.w}, wa[4 * t4 + k], a1);
      }
    }
    TS(2);
    f4 y;
#pragma unroll
    for (int i = 0; i < 4; ++i) { const auto s2 = __builtin_amdgcn_permlane32_swap(asu(a0[i]), asu(a1[i]), false, false); y[i] = asf(s2[0]) + dpp<R8>(asf(s2[1])); }
    const int abase = cp < 4 ? WS_QP + ks * 2 * QS + 48 * (4 * xg + cp) * 16 : WS_GU + (ks * 4 + cp - 4) * GS + 48 * xg * 16, abs_ = cp < 4 ? QS : 2 * GS;
    if (lb == 0 && hasA && (!(ABL & 2) || y.x == 1.2345f)) st4(rW, abase + (lane >> 5) * abs_ + (16 * sv + 8 * (g4 & 1) + e) * 16, y, epoch);
    TS(3);
    if (sv == 1 && hasF) {
      const unsigned tag2 = __builtin_amdgcn_perm(epoch, epoch, 0x01000100u);
      if (GUW) lds_wait(LU + L_FLAG + 1, 8); else lds_wait(LU + L_FLAG, 1);
      const int goff = WS_GU + ((lane >> 5) * 12 + lb * 2 + (g4 & 1)) * GS + (16 * (rg >> 1) + 8 * (e >> 2) + 4 * (rg & 1) + (e & 3)) * 16, g2 = lane < 32 ? goff + 8 * GS : goff;
      v4u gp[3] = {};
#pragma nounroll
      for (unsigned spin = 0; spin < ((ABL & 2) ? 0 : SPIN_CAP); ++spin) {
        MEMF();
        gp[0] = ldws(rW, goff); gp[1] = ldws(rW, goff, 4 * GS); gp[2] = ldws(rW, g2);
        if (!__any((bad4(gp[0], tag2) | bad4(gp[1], tag2) | bad4(gp[2], tag2)) != 0u)) break;
        __builtin_amdgcn_s_sleep(8);
      }
      f4 s4 = un4(gp[0]) + un4(gp[1]);
      if (lane < 32) s4 += un4(gp[2]);
#pragma unroll
      for (int i = 0; i < 4; ++i) s4[i] = sum32(s4[i]);
      const auto sw = __builtin_amdgcn_permlane16_swap(asu(lane < 32 ? s4.x : s4.y), asu(lane < 32 ? s4.z : s4.w), false, false);
      const float g = rbf(asf(sw[0])), u = rbf(asf(sw[1]));
      const float z = 0.7978845608028654f * (g + 0.044715f * g * g * g);
      const float th = 1.f - 2.f / (1.f + __expf(2.f * z));
      L[L_H + lb * 32 + 8 * (e & 3) + 4 * (e >> 2) + 2 * (g4 & 1) + (g4 >> 1)] = rbf(0.5f * g * (1.f + th)) * u;
      TS(5);
    }
    int ooff = WS_OP + (sp * 2 + b) * OS + h * OR + (16 * sv + j) * 16;
    int mofs = (L_PART + g4 * 4 * PST + 64 * sv + 4 * j) * 4, wofs = (L_FW + 2 * g4) * 4;
    asm volatile("" : "+v"(ooff), "+v"(mofs), "+v"(wofs));
    PADTO(1);
    BAR();
    TS(8);
    const f2 fa = *(LAS f2*)((LAS char*)L + wofs);
    f4 t[4];
#pragma unroll
    for (int i = 0; i < 4; ++i) t[i] = *(LAS f4*)((LAS char*)L + mofs + i * PST * 4);
    SB();
    f4 o = (t[0] + t[1]) * fa.x + (t[2] + t[3]) * fa.y;
#pragma unroll
    for (int i = 0; i < 4; ++i) o[i] = sum32(sum16(o[i]));
    if (lane < 16) st4(rW, ooff, o, epoch);
    TS(9);
  } else if (wv == 3) {
    unsigned epoch = __builtin_amdgcn_raw_buffer_load_b32(rW, WS_E, 0, SC1);
    BAR();
    EPOCH();
    const unsigned tag2 = __builtin_amdgcn_perm(epoch, epoch, 0x01000100u);
    while ((int)((unsigned)__builtin_amdgcn_s_memrealtime() - tstart) < QT) __builtin_amdgcn_s_sleep(4);
    const int qoff = WS_QP + b * QS + (48 * h + (lane < 48 ? lane : 0)) * 16;
    v4u qg[5] = {};
#pragma nounroll
    for (unsigned spin = 0; spin < ((ABL & 2) ? 0 : SPIN_CAP); ++spin) {
      MEMF();
#pragma unroll
      for (int i = 0; i < 5; ++i) qg[i] = ldws(rW, qoff, i * 2 * QS);
      if (!__any((bad4(qg[0], tag2) | bad4(qg[1], tag2) | bad4(qg[2], tag2) | bad4(qg[3], tag2) | bad4(qg[4], tag2)) != 0u)) break;
      __builtin_amdgcn_s_sleep(8);
    }
    const f4 s = ((un4(qg[0]) + un4(qg[1])) + (un4(qg[2]) + un4(qg[3]))) + un4(qg[4]);
    if (lane < 48) *(LAS v2u*)&LU[L_Q + 32 * g4 + 4 * e + 2 * lb] = v2u{pk(s.x, s.y), pk(s.z, s.w)};
    if (lane == 0) lds_post(LU + L_FLAG);
    TS(4);
    lds_wait(LU + L_FLAG + 1, 8);
    const f2 ml = *(LAS f2*)&L[L_ST + 2 * e];
    const float M = max8(ml.x), ex = __builtin_amdgcn_exp2f(ml.x - M), Ls = sum8(ml.y * ex);
    if (lane < 8) L[L_FW + lane] = ex / Ls;
    v4u lse = {asu(M + __builtin_amdgcn_logf(Ls)), epoch, 0u, epoch};
    TS(6);
    const int gq = 32 * rg + (lane & 31), gh = gq / 48;
    int loff = lane < 8 ? WS_OP + (sp * 2 + b) * OS + h * OR + 768 + lane * 16 : WS_END;
    int ooff = WS_OP + (lane >> 5) * OS + gh * OR + (gq - 48 * gh) * 16;
    int soff = WS_OP + ((lane & 3) * 2 + (lane >> 5)) * OS + gh * OR + 768;
    int foff = WS_FP + rg * FS + ((lane >> 5) * 160 + 32 * cg + (lane & 31)) * 16;
    int l16 = lane * 16;
    asm volatile("" : "+v"(ooff), "+v"(foff), "+v"(soff), "+v"(lse), "+v"(loff), "+v"(l16));
    PADTO(2);
    BAR();
    TS(8);
    __builtin_amdgcn_raw_buffer_store_b128(lse, rW, loff, 0, SC1);
    if (hasF) {
      v4u op[4] = {};
      v2u st = {};
      __builtin_amdgcn_s_sleep(PDELAY);
      if (!(ABL & 8)) sweep5(rW, ooff, soff, 2 * OS, 4 * OS, 6 * OS, tag2, epoch, op[0], op[1], op[2], op[3], st);
      TS(10);
      float Mg = asf(st.x);
      Mg = fmaxf(Mg, dpp<X1>(Mg)); Mg = fmaxf(Mg, dpp<X2>(Mg));
      float ws_ = __builtin_amdgcn_exp2f(asf(st.x) - Mg);
      ws_ *= __builtin_amdgcn_rcpf(sum4(ws_));
      *(LAS f4*)((LAS char*)L + L_O * 4 + l16) = (un4(op[0]) * dpp<0x00>(ws_) + un4(op[1]) * dpp<0x55>(ws_)) + (un4(op[2]) * dpp<0xAA>(ws_) + un4(op[3]) * dpp<0xFF>(ws_));
      BAR();
      BAR();
      f4 t[8];
#pragma unroll
      for (int i = 0; i < 8; ++i) t[i] = *(LAS f4*)((LAS char*)L + (L_FIN + i * 256) * 4 + l16);
      SB();
      st4(rW, foff, ((t[0] + t[1]) + (t[2] + t[3])) + ((t[4] + t[5]) + (t[6] + t[7])), epoch);
      TS(11);
    }
    QUIT();
  } else {
    const int w = wv - 4;
    const size_t kvb = ((size_t)(b * NH + h) * SK + (SK - WIN) + sp * 512) * DH;
    const rsrc rK = mkr(Kc + kvb, 512 * DH * 2), rV = mkr(Vc + kvb, 512 * DH * 2);
    const int kvo = w * 3072 + a * 384 + e * 16;
#define LDK(i) kb[i] = __builtin_amdgcn_raw_buffer_load_b128(rK, kvo + ((i) % 3) * 128, ((i) / 3) * 24576, NT)
#define LDV(i) vb[i] = __builtin_amdgcn_raw_buffer_load_b128(rV, kvo + ((i) % 3) * 128, ((i) / 3) * 24576, NT)
    v4u kb[24], vb[24];
#pragma unroll
    for (int i = 0; i < KPRE; ++i) LDK(i);
    BAR();
    const unsigned epoch = __builtin_amdgcn_raw_buffer_load_b32(rW, WS_E, 0, SC1) + 1u;
#pragma unroll
    for (int i = KPRE; i < QCHK; ++i) { PACE(i); LDK(i); }
    SB();
    TS(4);
    PADTO(3);
    if (__builtin_expect(*(volatile LAS unsigned*)(LU + L_FLAG) < 1u, 0)) lds_wait(LU + L_FLAG, 1);
    MEMF();
    TS(5);
    v4u qv[3];
#pragma unroll
    for (int c = 0; c < 3; ++c) qv[c] = *(LAS v4u*)&LU[L_Q + 32 * c + 4 * e];
    const rsrc rO = mkr(wo + (size_t)128 * rg * D + 128 * cg, hasF ? 128 * D * 2 : 0), rD = mkr(wd + (size_t)32 * rg * D + 128 * cg, hasF ? 32 * D * 2 : 0);
    int fo = (16 * w + 4 * g4) * 1280 + j * 16, fd = (4 * w + g4) * 1280 + j * 16;
    int ooff = (L_O + lb * 128 + 16 * w + 4 * g4) * 4;
    int pofs = ((w * 2 + lb) * PST + 8 * e + 4 * (g4 & 1)) * 4, fofs = (L_FIN + w * 256 + (g4 >> 1) * 128 + 8 * j + 4 * (g4 & 1)) * 4;
    unsigned tag2 = __builtin_amdgcn_perm(epoch, epoch, 0x01000100u);
    asm volatile("" : "+v"(fo), "+v"(fd), "+v"(ooff), "+v"(pofs), "+v"(fofs), "+v"(tag2));
    v4u fw[5];
    float sc[8];
    f4 o0 = {0.f, 0.f, 0.f, 0.f}, o1 = o0, o2 = o0;
#define QK(t) do { float d_ = 0.f; _Pragma("unroll") for (int c = 0; c < 3; ++c) _Pragma("unroll") for (int k = 0; k < 4; ++k) d_ = dot2(kb[3 * (t) + c][k], qv[c][k], d_); \
    sc[t] = rbf(rbf(sum8(d_)) * 0.07216878364870323f) * 1.4426950408889634f; asm volatile("" : "+v"(sc[t])); } while (0)
#define PV(t) do { const v4u ap_ = afrag(sc[t], msk); o0 = mfma(ap_, vb[3 * (t)], o0); o1 = mfma(ap_, vb[3 * (t) + 1], o1); o2 = mfma(ap_, vb[3 * (t) + 2], o2); } while (0)
#define LDF(i) fw[i] = (i) == 0 ? __builtin_amdgcn_raw_buffer_load_b128(rD, fd, 0, NT) : __builtin_amdgcn_raw_buffer_load_b128(rO, fo + ((i) - 1) * 1280, 0, NT)
#define VN(i) (24 + (i) + ((i) >= FPOS ? 5 : 0))
#define MX(a_, b_) ((a_) > (b_) ? (a_) : (b_))
#pragma unroll
    for (int n = QCHK; n < 53; ++n) {
      if (n <= VN(23) + W) PACE(n);
      if (n < 24) LDK(n < 24 ? n : 0);
      else if (n - 24 < FPOS) LDV(n >= 24 && n - 24 < FPOS && n < 48 ? n - 24 : 0);
      else if (n - 24 < FPOS + 5) LDF(n - 24 >= FPOS && n - 24 < FPOS + 5 ? n - 24 - FPOS : 0);
      else LDV(n - 29 >= 0 && n - 29 < 24 ? n - 29 : 0);
      SB();
#pragma unroll
      for (int t = 0; t < 8; ++t) if (n == MX(3 * t + 2 + W, QCHK + t)) QK(t);
      if (n == MX(23 + W, QCHK + 7)) {
        TS(6);
        float m = fmaxf(fmaxf(fmaxf(sc[0], sc[1]), fmaxf(sc[2], sc[3])), fmaxf(fmaxf(sc[4], sc[5]), fmaxf(sc[6], sc[7])));
        m = max32(max16(fmaxf(m, dpp<R8>(m))));
        float l = 0.f;
#pragma unroll
        for (int t = 0; t < 8; ++t) { sc[t] = __builtin_amdgcn_exp2f(sc[t] - m); l += sc[t]; }
        l = sum32(sum16(l + dpp<R8>(l)));
        if (lane == 0) { *(LAS f2*)&L[L_ST + 2 * w] = f2{m, l}; lds_post(LU + L_FLAG + 1); }
      }
#pragma unroll
      for (int t = 0; t < 7; ++t) if (n == MX(VN(3 * t + 2) + W, QCHK + 8 + t) || (n == 52 && VN(3 * t + 2) + W > 52)) PV(t);
      if (n == VN(23)) TS(7);
    }
    SB();
    PV(7);
    TS(8);
    if ((lane >> 5) == lb) {
      LAS char* p = (LAS char*)L + pofs;
      *(LAS f4*)(p) = o0; *(LAS f4*)(p + 256) = o1; *(LAS f4*)(p + 512) = o2;
    }
    BAR();
    if (hasF) {
      f4 acc = {0.f, 0.f, 0.f, 0.f}, acc2 = acc;
      acc = mfma(afrag(L[L_H + lb * 32 + 4 * w + g4], msk), fw[0], acc);
      asm volatile("" : "+v"(fw[1]), "+v"(fw[2]), "+v"(fw[3]), "+v"(fw[4]), "+v"(acc));
      TS(9);
      BAR();
      const f4 u = *(LAS f4*)((LAS char*)L + ooff);
      acc = mfma(afrag(u[0], msk), fw[1], acc);
      acc2 = mfma(afrag(u[1], msk), fw[2], acc2);
      acc = mfma(afrag(u[2], msk), fw[3], acc);
      acc2 = mfma(afrag(u[3], msk), fw[4], acc2);
      *(LAS f4*)((LAS char*)L + fofs) = acc + acc2;
      BAR();
      QUIT();
    } else if (w < 5) {
      const int rho = (xg * 2 + (r - 30)) * 5 + w;
      const int foff = WS_FP + (lane >> 2) * FS + (4 * rho + (lane & 3)) * 16;
      v2u xv = __builtin_amdgcn_raw_buffer_load_b64(rX, (4 * rho + (lane & 3)) * 8, 0, 0);
      asm volatile("" : "+v"(xv));
      v4u fp[3] = {};
      TS(12);
      if (!(ABL & 8)) sweep3(rW, foff, 16 * FS, 32 * FS, tag2, fp[0], fp[1], fp[2]);
      TS(13);
      f4 s = (un4(fp[0]) + un4(fp[1])) + un4(fp[2]);
#pragma unroll
      for (int i = 0; i < 4; ++i) { float t = s[i]; t += dpp<R4>(t); t += dpp<R8>(t); s[i] = sum32(sum16(t)); }
      if (lane < 4) ((v2u*)out)[4 * rho + lane] = v2u{pk(bflo(xv.x) + s.x, bfhi(xv.x) + s.y), pk(bflo(xv.y) + s.z, bfhi(xv.y) + s.w)};
      if (rho == 0 && lane == 0) __builtin_amdgcn_raw_buffer_store_b32(epoch, rW, WS_E, 0, SC1);
      TS(14);
      QUIT();
    }
  }
  FINISH();
}
}

extern "C" void kernel_launch(void* const* d_in, const int* in_sizes, int n_in, void* d_out, int out_size, void* d_ws, size_t ws_size, hipStream_t stream) {
  fused<<<256, 768, 0, stream>>>((const u16*)d_in[0], (const u16*)d_in[1], (const u16*)d_in[2], (const u16*)d_in[3], (const u16*)d_in[4],
                                 (const u16*)d_in[6], (const u16*)d_in[7], (const u16*)d_in[8], (u16*)d_out, (unsigned char*)d_ws);
}
```

```cpp
#include <hip/hip_runtime.h>
#include <hip/hip_bf16.h>
#include <stdint.h>

namespace {
typedef unsigned v4u __attribute__((ext_vector_type(4)));
typedef unsigned v2u __attribute__((ext_vector_type(2)));
typedef float f4 __attribute__((ext_vector_type(4)));
typedef float f2 __attribute__((ext_vector_type(2)));
typedef __bf16 bf8 __attribute__((ext_vector_type(8)));
typedef __bf16 bf2 __attribute__((ext_vector_type(2)));
typedef unsigned short u16;
#define GAS __attribute__((address_space(1)))
#define LAS __attribute__((address_space(3)))

constexpr int D = 640, NH = 32, SK = 8192, DH = 192, WIN = 2048, DF = 1536;
constexpr int NT = 2, SC1 = 16;
#define WND 4
#define STAMP 0
#define TAU 0
#define TOFF 0
#define ABL 0
#define FPOS 24
#define CAD 2
#define DEP 0
#define CAD3 2
#define DEP3 0
#define PDELAY 8
#define STSLEEP 16
#define QSLEEP 30
#define CODEPF 0
#define KPRE 1
constexpr int W = WND;
constexpr int WS_E = 0;
constexpr int QS = 1536 * 16 + 768, GS = 384 * 16 + 128, OS = 1536 * 16 + 512, FS = 320 * 16 + 128;
constexpr int WS_QP = 4096;
constexpr int WS_GU = WS_QP + 10 * QS;
constexpr int WS_ST = WS_GU + 20 * GS;
constexpr int WS_OP = WS_ST + 64 * 128;
constexpr int WS_FP = WS_OP + 8 * OS;
constexpr int WS_END = WS_FP + 48 * FS;
static_assert(WS_GU % 128 == 0 && WS_ST % 128 == 0 && WS_OP % 128 == 0 && WS_FP % 128 == 0, "line aligned");
constexpr int WS_STAMP = 4 << 20;
constexpr unsigned SPIN_CAP = 1u << 16;
constexpr int L_PART = 0, PST = 196;
constexpr int L_FIN = 16 * PST;
constexpr int L_Q = L_FIN + 2048;
constexpr int L_H = L_Q + 96;
constexpr int L_O = L_H + 64;
constexpr int L_ST = L_O + 256;
constexpr int L_FW = L_ST + 16;
constexpr int L_FLAG = L_FW + 8;
constexpr int L_END = L_FLAG + 8;

__device__ __forceinline__ float asf(unsigned u) { return __builtin_bit_cast(float, u); }
__device__ __forceinline__ unsigned asu(float f) { return __builtin_bit_cast(unsigned, f); }
__device__ __forceinline__ float bflo(unsigned u) { return asf(u << 16); }
__device__ __forceinline__ float bfhi(unsigned u) { return asf(u & 0xffff0000u); }
__device__ __forceinline__ unsigned pk(float lo, float hi) { bf2 r = {(__bf16)lo, (__bf16)hi}; return __builtin_bit_cast(unsigned, r); }
__device__ __forceinline__ float rbf(float f) { return (float)(__bf16)f; }
template <int CTRL> __device__ __forceinline__ float dpp(float x) { return asf(__builtin_amdgcn_mov_dpp(asu(x), CTRL, 0xf, 0xf, true)); }
constexpr int X1 = 0xB1, X2 = 0x4E, HM = 0x141, R4 = 0x124, R8 = 0x128;
__device__ __forceinline__ float sum16(float x) { auto s = __builtin_amdgcn_permlane16_swap(asu(x), asu(x), false, false); return asf(s[0]) + asf(s[1]); }
__device__ __forceinline__ float sum32(float x) { auto s = __builtin_amdgcn_permlane32_swap(asu(x), asu(x), false, false); return asf(s[0]) + asf(s[1]); }
__device__ __forceinline__ float max16(float x) { auto s = __builtin_amdgcn_permlane16_swap(asu(x), asu(x), false, false); return fmaxf(asf(s[0]), asf(s[1])); }
__device__ __forceinline__ float max32(float x) { auto s = __builtin_amdgcn_permlane32_swap(asu(x), asu(x), false, false); return fmaxf(asf(s[0]), asf(s[1])); }
__device__ __forceinline__ float sum4(float x) { x += dpp<X1>(x); x += dpp<X2>(x); return x; }
__device__ __forceinline__ float sum8(float x) { x = sum4(x); x += dpp<HM>(x); return x; }
__device__ __forceinline__ float max8(float x) { x = fmaxf(x, dpp<X1>(x)); x = fmaxf(x, dpp<X2>(x)); x = fmaxf(x, dpp<HM>(x)); return x; }
__device__ __forceinline__ float dot2(unsigned a, unsigned b, float c) { return __builtin_amdgcn_fdot2_f32_bf16(__builtin_bit_cast(bf2, a), __builtin_bit_cast(bf2, b), c, false); }
__device__ __forceinline__ f4 mfma(v4u a, v4u b, f4 c) { return __builtin_amdgcn_mfma_f32_16x16x32_bf16(__builtin_bit_cast(bf8, a), __builtin_bit_cast(bf8, b), c, 0, 0, 0); }
__device__ __forceinline__ v4u afrag(float w, v4u m) { const unsigned d = pk(w, w); return v4u{d & m.x, d & m.y, d & m.z, d & m.w}; }
typedef __amdgpu_buffer_rsrc_t rsrc;
__device__ __forceinline__ rsrc mkr(const void* p, int bytes) { return __builtin_amdgcn_make_buffer_rsrc((void*)p, 0, bytes, 0x00020000); }
#define SB() __builtin_amdgcn_sched_barrier(0)
#define MEMF() asm volatile("" ::: "memory")
#define BAR() do { SB(); asm volatile("s_waitcnt lgkmcnt(0)\n\ts_barrier" ::: "memory"); SB(); } while (0)
__device__ __forceinline__ v4u ldws(rsrc r, int off, int soff = 0) { return __builtin_amdgcn_raw_buffer_load_b128(r, off, soff, SC1); }
__device__ __forceinline__ void st4(rsrc r, int off, f4 v, unsigned tag) {
  const v4u p = {__builtin_amdgcn_perm(asu(v.x), asu(v.y), 0x07060503u), __builtin_amdgcn_perm(asu(v.y), tag, 0x06050100u),
                 __builtin_amdgcn_perm(asu(v.z), asu(v.w), 0x07060503u), __builtin_amdgcn_perm(asu(v.w), tag, 0x06050100u)};
  __builtin_amdgcn_raw_buffer_store_b128(p, r, off, 0, SC1);
}
__device__ __forceinline__ f4 un4(v4u p) {
  return f4{asf(p.x & 0xffffff00u), asf(__builtin_amdgcn_perm(p.x, p.y, 0x0403020cu)), asf(p.z & 0xffffff00u), asf(__builtin_amdgcn_perm(p.z, p.w, 0x0403020cu))};
}
__device__ __forceinline__ unsigned bad4(v4u p, unsigned tag2) { return __builtin_amdgcn_perm(p.y, p.w, 0x05040100u) ^ tag2; }
__device__ __forceinline__ void lds_wait(volatile LAS unsigned* f, unsigned want) {
  for (unsigned s = 0; *f < want && s < (SPIN_CAP << 4); ++s) __builtin_amdgcn_s_sleep(2);
  MEMF();
}
__device__ __forceinline__ void lds_post(LAS unsigned* f) { MEMF(); __hip_atomic_fetch_add(f, 1u, __ATOMIC_RELAXED, __HIP_MEMORY_SCOPE_WORKGROUP); }
__device__ __forceinline__ void sweep4(rsrc r, int off, int s1, int s2, int s3, unsigned tag2, v4u& p0, v4u& p1, v4u& p2, v4u& p3) {
  unsigned t0, t1, t2, cnt;
  p0 = p1 = p2 = p3 = v4u{0u, ~tag2, 0u, ~tag2};
#define CHK4 "v_perm_b32 %[t0], v65, v67, %[sel]\n\tv_perm_b32 %[t1], v69, v71, %[sel]\n\tv_perm_b32 %[t2], v73, v75, %[sel]\n\t" \
      "v_xor_b32 %[t0], %[t0], %[tag]\n\tv_xor_b32 %[t1], %[t1], %[tag]\n\tv_xor_b32 %[t2], %[t2], %[tag]\n\t" \
      "v_or3_b32 %[t0], %[t0], %[t1], %[t2]\n\tv_perm_b32 %[t1], v77, v79, %[sel]\n\tv_xor_b32 %[t1], %[t1], %[tag]\n\tv_or_b32 %[t0], %[t0], %[t1]\n\t" \
      "v_cmp_ne_u32 vcc, 0, %[t0]\n\ts_cbranch_vccnz 1b\n\t"
  asm volatile(
      "s_mov_b32 %[cnt], %[cap]\n"
      "1:\n\t"
      "buffer_load_dwordx4 v[64:67], %[off], %[r], 0 offen sc1\n\t"
      "buffer_load_dwordx4 v[68:71], %[off], %[r], %[s1] offen sc1\n\t"
      "buffer_load_dwordx4 v[72:75], %[off], %[r], %[s2] offen sc1\n\t"
      "buffer_load_dwordx4 v[76:79], %[off], %[r], %[s3] offen sc1\n\t"
      "s_sleep %[cad]\n\t"
      "s_waitcnt vmcnt(%[dep])\n\t"
      "s_sub_u32 %[cnt], %[cnt], 1\n\t"
      "s_cbranch_scc1 3f\n\t"
      CHK4
      "3:\n\t"
      "s_waitcnt vmcnt(0)"
      : "+{v[64:67]}"(p0), "+{v[68:71]}"(p1), "+{v[72:75]}"(p2), "+{v[76:79]}"(p3), [t0] "=&v"(t0), [t1] "=&v"(t1), [t2] "=&v"(t2), [cnt] "=&s"(cnt)
      : [off] "v"(off), [r] "s"(r), [s1] "s"(s1), [s2] "s"(s2), [s3] "s"(s3), [tag] "v"(tag2), [sel] "s"(0x05040100u), [cap] "n"(SPIN_CAP * 4), [cad] "n"(CAD), [dep] "n"(DEP)
      : "vcc", "scc", "memory");
}
__device__ __forceinline__ void sweep3(rsrc r, int off, int s1, int s2, unsigned tag2, v4u& p0, v4u& p1, v4u& p2) {
  unsigned t0, t1, t2, cnt;
  p0 = p1 = p2 = v4u{0u, ~tag2, 0u, ~tag2};
#define CHK3 "v_perm_b32 %[t0], v65, v67, %[sel]\n\tv_perm_b32 %[t1], v69, v71, %[sel]\n\tv_perm_b32 %[t2], v73, v75, %[sel]\n\t" \
      "v_xor_b32 %[t0], %[t0], %[tag]\n\tv_xor_b32 %[t1], %[t1], %[tag]\n\tv_xor_b32 %[t2], %[t2], %[tag]\n\t" \
      "v_or3_b32 %[t0], %[t0], %[t1], %[t2]\n\tv_cmp_ne_u32 vcc, 0, %[t0]\n\ts_cbranch_vccnz 1b\n\t"
  asm volatile(
      "s_mov_b32 %[cnt], %[cap]\n"
      "1:\n\t"
      "buffer_load_dwordx4 v[64:67], %[off], %[r], 0 offen sc1\n\t"
      "buffer_load_dwordx4 v[68:71], %[off], %[r], %[s1] offen sc1\n\t"
      "buffer_load_dwordx4 v[72:75], %[off], %[r], %[s2] offen sc1\n\t"
      "s_sleep %[cad]\n\t"
      "s_waitcnt vmcnt(%[dep])\n\t"
      "s_sub_u32 %[cnt], %[cnt], 1\n\t"
      "s_cbranch_scc1 3f\n\t"
      CHK3
      "3:\n\t"
      "s_waitcnt vmcnt(0)"
      : "+{v[64:67]}"(p0), "+{v[68:71]}"(p1), "+{v[72:75]}"(p2), [t0] "=&v"(t0), [t1] "=&v"(t1), [t2] "=&v"(t2), [cnt] "=&s"(cnt)
      : [off] "v"(off), [r] "s"(r), [s1] "s"(s1), [s2] "s"(s2), [tag] "v"(tag2), [sel] "s"(0x05040100u), [cap] "n"(SPIN_CAP * 4), [cad] "n"(CAD3), [dep] "n"(DEP3)
      : "vcc", "scc", "memory");
}
#define TS(i)
#define FINISH() __builtin_amdgcn_endpgm()
#define PACE(n) do { SB(); asm volatile("s_waitcnt vmcnt(%0)" :: "n"(W - 1)); SB(); } while (0)

__global__ void __launch_bounds__(768, 3) fused(const u16* __restrict__ x, const u16* __restrict__ Kc, const u16* __restrict__ Vc, const u16* __restrict__ wq,
                                                const u16* __restrict__ wo, const u16* __restrict__ wg, const u16* __restrict__ wu, const u16* __restrict__ wd,
                                                u16* __restrict__ out, unsigned char* ws) {
  __shared__ __attribute__((aligned(16))) float lds_[L_END];
  LAS float* const L = (LAS float*)lds_;
  LAS unsigned* const LU = (LAS unsigned*)lds_;
  const int tid = threadIdx.x, lane = tid & 63, wv = __builtin_amdgcn_readfirstlane(tid >> 6);
  TS(0);
  unsigned long long pc0;
  asm volatile("s_getpc_b64 %0" : "=s"(pc0));
  if (tid < 8) LU[L_FLAG + tid] = 0u;
  const int xg = blockIdx.x & 7, r = blockIdx.x >> 3;
  const int h = xg * 4 + (r >> 3), b = (r >> 2) & 1, sp = r & 3;
  const bool hasA = r >= 2, hasF = r < 30;
  const int rg = xg * 6 + r / 5, cg = r % 5;
  const int g4 = lane >> 4, j = lane & 15, lb = (lane >> 3) & 1, e = lane & 7, a = lane >> 3;
  asm volatile("" :: "s"(x), "s"(Kc), "s"(Vc), "s"(wq), "s"(wo), "s"(wg), "s"(wu), "s"(wd), "s"(out), "s"(ws));
  const rsrc rW = mkr(ws, WS_END);
  const rsrc rX = mkr(x, 2 * D * 2);
  const v4u msk = {(e >> 1) == 0 ? ((e & 1) ? 0xffff0000u : 0xffffu) : 0u, (e >> 1) == 1 ? ((e & 1) ? 0xffff0000u : 0xffffu) : 0u,
                   (e >> 1) == 2 ? ((e & 1) ? 0xffff0000u : 0xffffu) : 0u, (e >> 1) == 3 ? ((e & 1) ? 0xffff0000u : 0xffffu) : 0u};

  if (wv < 4) {
    const int sv = wv;
    unsigned epoch = __builtin_amdgcn_raw_buffer_load_b32(rW, WS_E, 0, SC1);
#define EPOCH() do { asm volatile("" : "+v"(epoch)); epoch += 1u; } while (0)
    if (sv < 3) {
      if (hasA) {
        const int ua = r - 2, ks = ua % 5, cp = ua / 5;
        const int jj = e | (g4 << 3);
        const v4u xs0 = __builtin_amdgcn_raw_buffer_load_b128(rX, lb * 1280 + jj * 16, 0, 0);
        const v4u xs1 = __builtin_amdgcn_raw_buffer_load_b128(rX, lb * 1280 + 512 + jj * 16, 0, 0);
        const v2u xs2 = __builtin_amdgcn_raw_buffer_load_b64(rX, lb * 1280 + 1024 + jj * 8, 0, 0);
        const int row0 = 128 * ks + 16 * a;
        v4u xr[2][2];
#pragma unroll
        for (int i = 0; i < 4; ++i) xr[i >> 1][i & 1] = __builtin_amdgcn_raw_buffer_load_b128(rX, row0 * 2 + (i >> 1) * 1280 + (i & 1) * 16, 0, 0);
        const int ldb = cp < 4 ? NH * DH * 2 : DF * 2;
        const rsrc rA = mkr(cp < 4 ? wq + 192 * (4 * xg + cp) : (cp == 4 ? wg : wu) + 192 * xg, (ABL & 1) ? 0 : (D - 1) * ldb + 384);
        const int ao = ((ABL & 16) ? (128 * ((ks + sv) % 5) + 16 * a) : (ABL & 32) ? 128 * ks + a : row0) * ldb + 128 * sv + 16 * e;
        v4u wa[16];
#pragma unroll
        for (int t = 0; t < 16; ++t) wa[t] = __builtin_amdgcn_raw_buffer_load_b128(rA, ao, ((ABL & 32) ? 8 * t : t) * ldb, NT);
        BAR();
        EPOCH();
        float v[20];
#pragma unroll
        for (int i = 0; i < 4; ++i) { v[2 * i] = bflo(xs0[i]); v[2 * i + 1] = bfhi(xs0[i]); v[8 + 2 * i] = bflo(xs1[i]); v[9 + 2 * i] = bfhi(xs1[i]); }
        v[16] = bflo(xs2[0]); v[17] = bfhi(xs2[0]); v[18] = bflo(xs2[1]); v[19] = bfhi(xs2[1]);
        float s = 0.f;
#pragma unroll
        for (int i = 0; i < 20; ++i) s += v[i];
        s = sum32(sum16(sum8(s)));
        const float mean = s * (1.f / D);
        float q = 0.f;
#pragma unroll
        for (int i = 0; i < 20; ++i) { const float d = v[i] - mean; q = fmaf(d, d, q); }
        q = sum32(sum16(sum8(q)));
        const float mb = rbf(mean), rs = 1.f / rbf(sqrtf(rbf(rbf(q * (1.f / D)) + 1e-5f)));
        const float mb0 = asf(__builtin_amdgcn_readlane(asu(mb), 0)), rs0 = asf(__builtin_amdgcn_readlane(asu(rs), 0));
        const float mb1 = asf(__builtin_amdgcn_readlane(asu(mb), 8)), rs1 = asf(__builtin_amdgcn_readlane(asu(rs), 8));
        TS(1);
        f4 a0 = {0.f, 0.f, 0.f, 0.f}, a1 = a0;
#pragma unroll
        for (int t = 0; t < 16; ++t) {
          const unsigned u0 = xr[0][t >> 3][(t >> 1) & 3], u1 = xr[1][t >> 3][(t >> 1) & 3];
          a0 = mfma(afrag(rbf(((t & 1) ? bfhi(u0) : bflo(u0)) - mb0) * rs0, msk), wa[t], a0);
          a1 = mfma(afrag(rbf(((t & 1) ? bfhi(u1) : bflo(u1)) - mb1) * rs1, msk), wa[t], a1);
        }
        TS(2);
        f4 y;
#pragma unroll
        for (int i = 0; i < 4; ++i) { const auto s2 = __builtin_amdgcn_permlane32_swap(asu(a0[i]), asu(a1[i]), false, false); y[i] = asf(s2[0]) + dpp<R8>(asf(s2[1])); }
        const int off = (cp < 4 ? WS_QP + (ks * 2 + (lane >> 5)) * QS + 48 * (4 * xg + cp) * 16 : WS_GU + ((ks * 2 + (lane >> 5)) * 2 + (cp - 4)) * GS + 48 * xg * 16) +
                        (16 * sv + 2 * e + (g4 & 1)) * 16;
        if (lb == 0 && (!(ABL & 2) || y.x == 1.2345f)) st4(rW, off, y, epoch);
        __builtin_amdgcn_s_sleep(QSLEEP);
      } else {
        BAR();
        EPOCH();
        __builtin_amdgcn_s_sleep(120);
      }
      TS(3);
      const unsigned tag2 = __builtin_amdgcn_perm(epoch, epoch, 0x01000100u);
      if (sv == 0) {
        const int qoff = WS_QP + b * QS + (48 * h + (lane < 48 ? lane : 0)) * 16;
        v4u qg[5] = {};
        for (unsigned spin = 0; spin < ((ABL & 2) ? 0 : SPIN_CAP); ++spin) {
          MEMF();
#pragma unroll
          for (int i = 0; i < 5; ++i) qg[i] = ldws(rW, qoff, i * 2 * QS);
          if (!__any((bad4(qg[0], tag2) | bad4(qg[1], tag2) | bad4(qg[2], tag2) | bad4(qg[3], tag2) | bad4(qg[4], tag2)) != 0u)) break;
          __builtin_amdgcn_s_sleep(8);
        }
        const f4 s = ((un4(qg[0]) + un4(qg[1])) + (un4(qg[2]) + un4(qg[3]))) + un4(qg[4]);
        if (lane < 48) *(LAS v2u*)&LU[L_Q + 2 * lane] = v2u{pk(s.x, s.y), pk(s.z, s.w)};
        if (lane == 0) lds_post(LU + L_FLAG);
      } else if (sv == 1 && hasF) {
        lds_wait(LU + L_FLAG, 1);
        const int goff = WS_GU + (lb * 2 + (g4 & 1)) * GS + (8 * rg + e) * 16;
        v4u gp[5] = {};
        for (unsigned spin = 0; spin < ((ABL & 2) ? 0 : SPIN_CAP); ++spin) {
          MEMF();
#pragma unroll
          for (int i = 0; i < 5; ++i) gp[i] = ldws(rW, goff, i * 4 * GS);
          if (!__any((bad4(gp[0], tag2) | bad4(gp[1], tag2) | bad4(gp[2], tag2) | bad4(gp[3], tag2) | bad4(gp[4], tag2)) != 0u)) break;
          __builtin_amdgcn_s_sleep(8);
        }
        const f4 s = ((un4(gp[0]) + un4(gp[1])) + (un4(gp[2]) + un4(gp[3]))) + un4(gp[4]);
        const auto sw = __builtin_amdgcn_permlane16_swap(asu(lane < 32 ? s.x : s.y), asu(lane < 32 ? s.z : s.w), false, false);
        const float g = rbf(asf(sw[0])), u = rbf(asf(sw[1]));
        const float z = 0.7978845608028654f * (g + 0.044715f * g * g * g);
        const float th = 1.f - 2.f / (1.f + __expf(2.f * z));
        L[L_H + lb * 32 + 4 * e + 2 * (g4 & 1) + (g4 >> 1)] = rbf(0.5f * g * (1.f + th)) * u;
      }
      TS(4);
    } else {
      BAR();
      EPOCH();
      lds_wait(LU + L_FLAG + 1, 8);
      const f2 ml = *(LAS f2*)&L[L_ST + 2 * e];
      const float M = max8(ml.x), Ls = sum8(ml.y * __builtin_amdgcn_exp2f(ml.x - M));
      const int stoff = WS_ST + (b * NH + h) * 128;
      if (lane == 0 && !(ABL & 4)) __builtin_amdgcn_raw_buffer_store_b128(v4u{asu(M), epoch, asu(Ls), epoch}, rW, stoff + sp * 16, 0, SC1);
      TS(5);
      v4u stp = {asu(M), 0, asu(Ls), 0};
      for (unsigned spin = 0; spin < ((ABL & 4) ? 0 : SPIN_CAP); ++spin) {
        __builtin_amdgcn_s_sleep(STSLEEP);
        MEMF();
        stp = ldws(rW, stoff + (lane & 3) * 16);
        if (!__any(((stp.y ^ epoch) | (stp.w ^ epoch)) != 0u)) break;
      }
      float Mg = asf(stp.x);
      Mg = fmaxf(Mg, dpp<X1>(Mg)); Mg = fmaxf(Mg, dpp<X2>(Mg));
      const float Lg = sum4(asf(stp.z) * __builtin_amdgcn_exp2f(asf(stp.x) - Mg));
      if (lane < 8) L[L_FW + lane] = __builtin_amdgcn_exp2f(ml.x - Mg) / Lg;
      TS(6);
    }
    int ooff = sv < 3 ? WS_OP + (sp * 2 + b) * OS + (48 * h + (tid >> 2)) * 16 : WS_OP + (lane >> 5) * OS + (32 * rg + (lane & 31)) * 16;
    int foff = WS_FP + rg * FS + ((lane >> 5) * 160 + 32 * cg + (lane & 31)) * 16;
    int mofs = (L_PART + (tid & 3) * 4 * PST + (tid & ~3)) * 4;
    asm volatile("" : "+v"(ooff), "+v"(foff), "+v"(mofs));
    BAR();
    TS(8);
    if (sv < 3) {
      const f2 fa = *(LAS f2*)&L[L_FW + 2 * (tid & 3)];
      f4 t[4];
#pragma unroll
      for (int i = 0; i < 4; ++i) t[i] = *(LAS f4*)((LAS char*)L + mofs + i * PST * 4);
      SB();
      f4 s = (t[0] + t[1]) * fa.x + (t[2] + t[3]) * fa.y;
#pragma unroll
      for (int i = 0; i < 4; ++i) s[i] = sum4(s[i]);
      if (!(lane & 3)) st4(rW, ooff, s, epoch);
      TS(9);
      FINISH();
    }
    if (hasF) {
      v4u op[4] = {};
      __builtin_amdgcn_s_sleep(PDELAY);
      if (!(ABL & 8)) sweep4(rW, ooff, 2 * OS, 4 * OS, 6 * OS, __builtin_amdgcn_perm(epoch, epoch, 0x01000100u), op[0], op[1], op[2], op[3]);
      TS(10);
      *(LAS f4*)&L[L_O + 4 * lane] = (un4(op[0]) + un4(op[1])) + (un4(op[2]) + un4(op[3]));
      BAR();
      BAR();
      f4 t[8];
#pragma unroll
      for (int i = 0; i < 8; ++i) t[i] = *(LAS f4*)&L[L_FIN + i * 256 + 4 * lane];
      SB();
      st4(rW, foff, ((t[0] + t[1]) + (t[2] + t[3])) + ((t[4] + t[5]) + (t[6] + t[7])), epoch);
      TS(11);
    }
    FINISH();
  }
  {
    const int w = wv - 4;
    const size_t kvb = ((size_t)(b * NH + h) * SK + (SK - WIN) + sp * 512) * DH;
    const rsrc rK = mkr(Kc + kvb, 512 * DH * 2), rV = mkr(Vc + kvb, 512 * DH * 2);
    const int kvo = w * 3072 + a * 384 + e * 16;
#define LDK(i) kb[i] = __builtin_amdgcn_raw_buffer_load_b128(rK, kvo + ((i) % 3) * 128, ((i) / 3) * 24576, NT)
#define LDV(i) vb[i] = __builtin_amdgcn_raw_buffer_load_b128(rV, kvo + ((i) % 3) * 128, ((i) / 3) * 24576, NT)
    v4u kb[24], vb[24];
#pragma unroll
    for (int i = 0; i < KPRE; ++i) LDK(i);
    BAR();
    const unsigned epoch = __builtin_amdgcn_raw_buffer_load_b32(rW, WS_E, 0, SC1) + 1u;
#pragma unroll
    for (int i = KPRE; i < 15; ++i) { PACE(i); LDK(i); }
    SB();
    TS(4);
    if (__builtin_expect(*(volatile LAS unsigned*)(LU + L_FLAG) < 1u, 0)) lds_wait(LU + L_FLAG, 1);
    MEMF();
    TS(5);
    v4u qv[3];
#pragma unroll
    for (int c = 0; c < 3; ++c) qv[c] = *(LAS v4u*)&LU[L_Q + 32 * c + 4 * e];
    const rsrc rO = mkr(wo + (size_t)128 * rg * D + 128 * cg, hasF ? 128 * D * 2 : 0), rD = mkr(wd + (size_t)32 * rg * D + 128 * cg, hasF ? 32 * D * 2 : 0);
    int fo = (16 * w + 4 * g4) * 1280 + j * 16, fd = (4 * w + g4) * 1280 + j * 16;
    int ooff = (L_O + lb * 128 + 16 * w + 4 * g4) * 4;
    int pofs = ((w * 2 + lb) * PST + 8 * e + 4 * (g4 & 1)) * 4, fofs = (L_FIN + w * 256 + (g4 >> 1) * 128 + 8 * j + 4 * (g4 & 1)) * 4;
    unsigned tag2 = __builtin_amdgcn_perm(epoch, epoch, 0x01000100u);
    asm volatile("" : "+v"(fo), "+v"(fd), "+v"(ooff), "+v"(pofs), "+v"(fofs), "+v"(tag2));
    v4u fw[5];
    float sc[8];
    f4 o0 = {0.f, 0.f, 0.f, 0.f}, o1 = o0, o2 = o0;
#define QK(t) do { float d_ = 0.f; _Pragma("unroll") for (int c = 0; c < 3; ++c) _Pragma("unroll") for (int k = 0; k < 4; ++k) d_ = dot2(kb[3 * (t) + c][k], qv[c][k], d_); \
    sc[t] = rbf(rbf(sum8(d_)) * 0.07216878364870323f) * 1.4426950408889634f; asm volatile("" : "+v"(sc[t])); } while (0)
#define PV(t) do { const v4u ap_ = afrag(sc[t], msk); o0 = mfma(ap_, vb[3 * (t)], o0); o1 = mfma(ap_, vb[3 * (t) + 1], o1); o2 = mfma(ap_, vb[3 * (t) + 2], o2); } while (0)
#define LDF(i) fw[i] = (i) == 0 ? __builtin_amdgcn_raw_buffer_load_b128(rD, fd, 0, NT) : __builtin_amdgcn_raw_buffer_load_b128(rO, fo + ((i) - 1) * 1280, 0, NT)
#define VN(i) (24 + (i) + ((i) >= FPOS ? 5 : 0))
#define MX(a_, b_) ((a_) > (b_) ? (a_) : (b_))
#pragma unroll
    for (int n = 15; n < 53; ++n) {
      if (n <= VN(23) + W) PACE(n);
      if (n < 24) LDK(n < 24 ? n : 0);
      else if (n - 24 < FPOS) LDV(n >= 24 && n - 24 < FPOS && n < 48 ? n - 24 : 0);
      else if (n - 24 < FPOS + 5) LDF(n - 24 >= FPOS && n - 24 < FPOS + 5 ? n - 24 - FPOS : 0);
      else LDV(n - 29 >= 0 && n - 29 < 24 ? n - 29 : 0);
      SB();
#pragma unroll
      for (int t = 0; t < 8; ++t) if (n == MX(3 * t + 2 + W, 15 + t)) QK(t);
      if (n == 23 + W) {
        TS(6);
        float m = fmaxf(fmaxf(fmaxf(sc[0], sc[1]), fmaxf(sc[2], sc[3])), fmaxf(fmaxf(sc[4], sc[5]), fmaxf(sc[6], sc[7])));
        m = max32(max16(fmaxf(m, dpp<R8>(m))));
        float l = 0.f;
#pragma unroll
        for (int t = 0; t < 8; ++t) { sc[t] = __builtin_amdgcn_exp2f(sc[t] - m); l += sc[t]; }
        l = sum32(sum16(l + dpp<R8>(l)));
        if (lane == 0) { *(LAS f2*)&L[L_ST + 2 * w] = f2{m, l}; lds_post(LU + L_FLAG + 1); }
      }
#pragma unroll
      for (int t = 0; t < 7; ++t) if (n == VN(3 * t + 2) + W || (n == 52 && VN(3 * t + 2) + W > 52)) PV(t);
      if (n == VN(23)) TS(7);
    }
    SB();
    PV(7);
    TS(8);
    if ((lane >> 5) == lb) {
      LAS char* p = (LAS char*)L + pofs;
      *(LAS f4*)(p) = o0; *(LAS f4*)(p + 256) = o1; *(LAS f4*)(p + 512) = o2;
    }
    BAR();
    if (hasF) {
      f4 acc = {0.f, 0.f, 0.f, 0.f}, acc2 = acc;
      acc = mfma(afrag(L[L_H + lb * 32 + 4 * w + g4], msk), fw[0], acc);
      asm volatile("" : "+v"(fw[1]), "+v"(fw[2]), "+v"(fw[3]), "+v"(fw[4]), "+v"(acc));
      TS(9);
      BAR();
      const f4 u = *(LAS f4*)((LAS char*)L + ooff);
      acc = mfma(afrag(u[0], msk), fw[1], acc);
      acc2 = mfma(afrag(u[1], msk), fw[2], acc2);
      acc = mfma(afrag(u[2], msk), fw[3], acc);
      acc2 = mfma(afrag(u[3], msk), fw[4], acc2);
      *(LAS f4*)((LAS char*)L + fofs) = acc + acc2;
      BAR();
      FINISH();
    }
    if (w < 5) {
      const int rho = (xg * 2 + (r - 30)) * 5 + w;
      const int foff = WS_FP + (lane >> 2) * FS + (4 * rho + (lane & 3)) * 16;
      v2u xv = __builtin_amdgcn_raw_buffer_load_b64(rX, (4 * rho + (lane & 3)) * 8, 0, 0);
      asm volatile("" : "+v"(xv));
      v4u fp[3] = {};
      TS(12);
      if (!(ABL & 8)) sweep3(rW, foff, 16 * FS, 32 * FS, tag2, fp[0], fp[1], fp[2]);
      TS(13);
      f4 s = (un4(fp[0]) + un4(fp[1])) + un4(fp[2]);
#pragma unroll
      for (int i = 0; i < 4; ++i) { float t = s[i]; t += dpp<R4>(t); t += dpp<R8>(t); s[i] = sum32(sum16(t)); }
      if (lane < 4) ((v2u*)out)[4 * rho + lane] = v2u{pk(bflo(xv.x) + s.x, bfhi(xv.x) + s.y), pk(bflo(xv.y) + s.z, bfhi(xv.y) + s.w)};
      if (rho == 0 && lane == 0) __builtin_amdgcn_raw_buffer_store_b32(epoch, rW, WS_E, 0, SC1);
      TS(14);
    }
  }
  FINISH();
}
}

extern "C" void kernel_launch(void* const* d_in, const int* in_sizes, int n_in, void* d_out, int out_size, void* d_ws, size_t ws_size, hipStream_t stream) {
  fused<<<256, 768, 0, stream>>>((const u16*)d_in[0], (const u16*)d_in[1], (const u16*)d_in[2], (const u16*)d_in[3], (const u16*)d_in[4],
                                 (const u16*)d_in[6], (const u16*)d_in[7], (const u16*)d_in[8], (u16*)d_out, (unsigned char*)d_ws);
}
```

```cpp
#include <hip/hip_runtime.h>
#include <hip/hip_bf16.h>
#include <stdint.h>

namespace {
typedef unsigned v4u __attribute__((ext_vector_type(4)));
typedef unsigned v2u __attribute__((ext_vector_type(2)));
typedef float f4 __attribute__((ext_vector_type(4)));
typedef float f2 __attribute__((ext_vector_type(2)));
typedef short s4 __attribute__((ext_vector_type(4)));
typedef __bf16 bf8 __attribute__((ext_vector_type(8)));
typedef __bf16 bf2 __attribute__((ext_vector_type(2)));
typedef unsigned short u16;
typedef unsigned long long u64;
#define LAS __attribute__((address_space(3)))

constexpr int D = 640, NH = 32, SK = 8192, DH = 192, WIN = 2048, DF = 1536;
constexpr int NT = 2, SC1 = 16;
#define DEF(n, v)
#define WND 3
#define STAMP 0
#define TAU 0
#define TOFF 170
#define ABL 0
#define CAD 0
#define CAD3 0
#define PDELAY 26
#define RDELAY 14
#define QT 650
#define QCHK 23
#define PAD1 0
#define PAD2 0
#define PAD3 0
#define TGP 0
#define TGQ 0
#define MASKED 1
#define OUTM 1
#define OAUX 16
#define OPRE 0
#define NOE 0
#define EAUX 16
#define TGR 0
#define DEP 0
#define DEP3 0
#define LSEN 42
constexpr int W = WND;
constexpr int WS_E = 0;
constexpr int QS = 6144 * 4 + 768, GS = 1536 * 4 + 128, OS = 6144 * 4 + 512, FS = 1280 * 4 + 128;
constexpr int WS_QP = 4096;
constexpr int WS_GU = WS_QP + 10 * QS;
constexpr int WS_ST = WS_GU + 20 * GS;
constexpr int WS_OP = WS_ST + 256 * 128;
constexpr int WS_FP = WS_OP + 8 * OS;
constexpr int WS_END = WS_FP + 48 * FS;
static_assert(WS_GU % 128 == 0 && WS_ST % 128 == 0 && WS_OP % 128 == 0 && WS_FP % 128 == 0, "line aligned");
constexpr int WS_STAMP = 4 << 20;
constexpr unsigned SPIN_CAP = 1u << 16;
constexpr int L_PART = 0, PST = 196;
constexpr int L_Q = 16 * PST;
constexpr int L_O = L_Q + 96;
constexpr int L_ST = L_O + 160;
constexpr int L_FW = L_ST + 16;
constexpr int L_FLAG = L_FW + 8;
constexpr int L_XN = L_FLAG + 8;
constexpr int L_A = L_XN + 1280;
constexpr int L_END = L_A + 48 * 256;
constexpr int FRS = 288;

__device__ __forceinline__ float asf(unsigned u) { return __builtin_bit_cast(float, u); }
__device__ __forceinline__ unsigned asu(float f) { return __builtin_bit_cast(unsigned, f); }
__device__ __forceinline__ float bflo(unsigned u) { return asf(u << 16); }
__device__ __forceinline__ float bfhi(unsigned u) { return asf(u & 0xffff0000u); }
__device__ __forceinline__ unsigned pk(float lo, float hi) { bf2 r = {(__bf16)lo, (__bf16)hi}; return __builtin_bit_cast(unsigned, r); }
__device__ __forceinline__ float rbf(float f) { return (float)(__bf16)f; }
template <int CTRL> __device__ __forceinline__ float dpp(float x) { return asf(__builtin_amdgcn_mov_dpp(asu(x), CTRL, 0xf, 0xf, true)); }
constexpr int X1 = 0xB1, X2 = 0x4E, HM = 0x141, R4 = 0x124, R8 = 0x128;
__device__ __forceinline__ float swadd16(float x, float y) { auto s = __builtin_amdgcn_permlane16_swap(asu(x), asu(y), false, false); return asf(s[0]) + asf(s[1]); }
__device__ __forceinline__ float swadd32(float x, float y) { auto s = __builtin_amdgcn_permlane32_swap(asu(x), asu(y), false, false); return asf(s[0]) + asf(s[1]); }
__device__ __forceinline__ float sum16(float x) { return swadd16(x, x); }
__device__ __forceinline__ float sum32(float x) { return swadd32(x, x); }
__device__ __forceinline__ float rowsum4(f4 v) { return swadd16(swadd32(v.x, v.y), swadd32(v.z, v.w)); }
__device__ __forceinline__ float max16(float x) { auto s = __builtin_amdgcn_permlane16_swap(asu(x), asu(x), false, false); return fmaxf(asf(s[0]), asf(s[1])); }
__device__ __forceinline__ float max32(float x) { auto s = __builtin_amdgcn_permlane32_swap(asu(x), asu(x), false, false); return fmaxf(asf(s[0]), asf(s[1])); }
__device__ __forceinline__ float sum4(float x) { x += dpp<X1>(x); x += dpp<X2>(x); return x; }
__device__ __forceinline__ float sum8(float x) { x = sum4(x); x += dpp<HM>(x); return x; }
__device__ __forceinline__ float max8(float x) { x = fmaxf(x, dpp<X1>(x)); x = fmaxf(x, dpp<X2>(x)); x = fmaxf(x, dpp<HM>(x)); return x; }
__device__ __forceinline__ float dot2(unsigned a, unsigned b, float c) { return __builtin_amdgcn_fdot2_f32_bf16(__builtin_bit_cast(bf2, a), __builtin_bit_cast(bf2, b), c, false); }
__device__ __forceinline__ f4 mfma(v4u a, v4u b, f4 c) { return __builtin_amdgcn_mfma_f32_16x16x32_bf16(__builtin_bit_cast(bf8, a), __builtin_bit_cast(bf8, b), c, 0, 0, 0); }
__device__ __forceinline__ v4u afrag(float w, v4u m) { const unsigned d = pk(w, w); return v4u{d & m.x, d & m.y, d & m.z, d & m.w}; }
typedef __amdgpu_buffer_rsrc_t rsrc;
__device__ __forceinline__ rsrc mkr(const void* p, int bytes) { return __builtin_amdgcn_make_buffer_rsrc((void*)p, 0, bytes, 0x00020000); }
#define SB() __builtin_amdgcn_sched_barrier(0)
#define MEMF() asm volatile("" ::: "memory")
#define BAR() do { SB(); asm volatile("s_waitcnt lgkmcnt(0)\n\ts_barrier" ::: "memory"); SB(); } while (0)
__device__ __forceinline__ v4u ldws(rsrc r, int off, int soff = 0) { return __builtin_amdgcn_raw_buffer_load_b128(r, off, soff, SC1); }
__device__ __forceinline__ unsigned tagged(float v, unsigned tag) { return __builtin_amdgcn_perm(asu(v), tag, 0x07060500u); }
__device__ __forceinline__ f4 un4(v4u p) { return f4{asf(p.x & 0xffffff00u), asf(p.y & 0xffffff00u), asf(p.z & 0xffffff00u), asf(p.w & 0xffffff00u)}; }
__device__ __forceinline__ unsigned bad4(v4u p, unsigned tag4) { return (__builtin_amdgcn_perm(p.x, p.y, 0x0c0c0400u) | __builtin_amdgcn_perm(p.z, p.w, 0x04000c0cu)) ^ tag4; }
__device__ __forceinline__ void lds_wait(volatile LAS unsigned* f, unsigned want) {
  for (unsigned s = 0; *f < want && s < (SPIN_CAP << 4); ++s) __builtin_amdgcn_s_sleep(2);
  MEMF();
}
__device__ __forceinline__ void lds_post(LAS unsigned* f) { asm volatile("s_mov_b64 exec, 1\n\tds_add_u32 %0, %1\n\ts_mov_b64 exec, -1" :: "v"((unsigned)(size_t)f), "v"(1u) : "memory"); }
#define LDP(m, regs, so) "s_mov_b64 exec, %[" #m "]\n\tbuffer_load_dwordx4 " regs ", %[off], %[r], " so " offen sc1\n\t"
#define CKP(m, a, b, c, d) "v_perm_b32 %[t0], " a ", " b ", %[sa]\n\tv_perm_b32 %[t1], " c ", " d ", %[sb]\n\tv_or_b32 %[t0], %[t0], %[t1]\n\tv_cmp_ne_u32 %[" #m "], %[t0], %[tag]\n\t"
#define WQM(m) "s_wqm_b64 %[" #m "], %[" #m "]\n\t"
__device__ __forceinline__ void sweep4(rsrc r, int off, int s1, int s2, int s3, unsigned tag4, v4u& p0, v4u& p1, v4u& p2, v4u& p3) {
  unsigned t0, t1, cnt;
  u64 m0, m1, m2, m3;
  p0 = p1 = p2 = p3 = v4u{~tag4, ~tag4, ~tag4, ~tag4};
  asm volatile(
      "s_mov_b32 %[cnt], %[cap]\n\t"
      "s_mov_b64 %[m0], -1\n\ts_mov_b64 %[m1], -1\n\ts_mov_b64 %[m2], -1\n\ts_mov_b64 %[m3], -1\n\t"
      "s_branch 2f\n"
      "1:\n\t"
      "s_sleep %[cad]\n\t"
      WQM(m0) WQM(m1) WQM(m2) WQM(m3)
      "s_sub_u32 %[cnt], %[cnt], 1\n\t"
      "s_cbranch_scc1 3f\n"
      "2:\n\t"
      LDP(m0, "v[64:67]", "0") LDP(m1, "v[68:71]", "%[s1]") LDP(m2, "v[72:75]", "%[s2]") LDP(m3, "v[76:79]", "%[s3]")
      "s_mov_b64 exec, -1\n\t"
      "s_waitcnt vmcnt(%[dep])\n\t"
      CKP(m0, "v64", "v65", "v66", "v67") CKP(m1, "v68", "v69", "v70", "v71") CKP(m2, "v72", "v73", "v74", "v75") CKP(m3, "v76", "v77", "v78", "v79")
      "s_or_b64 vcc, %[m0], %[m1]\n\ts_or_b64 vcc, vcc, %[m2]\n\ts_or_b64 vcc, vcc, %[m3]\n\t"
      "s_cbranch_scc1 1b\n"
      "3:"
      : "+&{v[64:67]}"(p0), "+&{v[68:71]}"(p1), "+&{v[72:75]}"(p2), "+&{v[76:79]}"(p3), [t0] "=&v"(t0), [t1] "=&v"(t1), [cnt] "=&s"(cnt),
        [m0] "=&s"(m0), [m1] "=&s"(m1), [m2] "=&s"(m2), [m3] "=&s"(m3)
      : [off] "v"(off), [r] "s"(r), [s1] "s"(s1), [s2] "s"(s2), [s3] "s"(s3), [tag] "v"(tag4), [sa] "s"(0x0c0c0400u), [sb] "s"(0x04000c0cu),
        [cap] "n"(SPIN_CAP * 4), [cad] "n"(CAD), [dep] "n"(DEP)
      : "vcc", "scc", "memory");
}
__device__ __forceinline__ void sweep3(rsrc r, int off, int s1, int s2, unsigned tag4, v4u& p0, v4u& p1, v4u& p2) {
  unsigned t0, t1, cnt;
  u64 m0, m1, m2;
  p0 = p1 = p2 = v4u{~tag4, ~tag4, ~tag4, ~tag4};
  asm volatile(
      "s_mov_b32 %[cnt], %[cap]\n\t"
      "s_mov_b64 %[m0], -1\n\ts_mov_b64 %[m1], -1\n\ts_mov_b64 %[m2], -1\n\t"
      "s_branch 2f\n"
      "1:\n\t"
      "s_sleep %[cad]\n\t"
      WQM(m0) WQM(m1) WQM(m2)
      "s_sub_u32 %[cnt], %[cnt], 1\n\t"
      "s_cbranch_scc1 3f\n"
      "2:\n\t"
      LDP(m0, "v[64:67]", "0") LDP(m1, "v[68:71]", "%[s1]") LDP(m2, "v[72:75]", "%[s2]")
      "s_mov_b64 exec, -1\n\t"
      "s_waitcnt vmcnt(%[dep])\n\t"
      CKP(m0, "v64", "v65", "v66", "v67") CKP(m1, "v68", "v69", "v70", "v71") CKP(m2, "v72", "v73", "v74", "v75")
      "s_or_b64 vcc, %[m0], %[m1]\n\ts_or_b64 vcc, vcc, %[m2]\n\t"
      "s_cbranch_scc1 1b\n"
      "3:"
      : "+&{v[64:67]}"(p0), "+&{v[68:71]}"(p1), "+&{v[72:75]}"(p2), [t0] "=&v"(t0), [t1] "=&v"(t1), [cnt] "=&s"(cnt), [m0] "=&s"(m0), [m1] "=&s"(m1), [m2] "=&s"(m2)
      : [off] "v"(off), [r] "s"(r), [s1] "s"(s1), [s2] "s"(s2), [tag] "v"(tag4), [sa] "s"(0x0c0c0400u), [sb] "s"(0x04000c0cu), [cap] "n"(SPIN_CAP * 4), [cad] "n"(CAD3), [dep] "n"(DEP3)
      : "vcc", "scc", "memory");
}
#define TS(i)
#define FINISH()
#define QUIT() asm volatile("s_endpgm" ::: "memory")
#define PADTO(k) do { if (PAD##k) asm volatile("s_branch 1f\n\t.fill %0, 4, 0xbf800000\n1:" :: "n"(PAD##k / 4)); } while (0)
#define PACE(n) do { SB(); asm volatile("s_waitcnt vmcnt(%0)" :: "n"(W - 1)); SB(); } while (0)

__global__ void __launch_bounds__(768, 3) __attribute__((aligned(4096))) fused(const u16* __restrict__ x, const u16* __restrict__ Kc, const u16* __restrict__ Vc,
    const u16* __restrict__ wq, const u16* __restrict__ wo, const u16* __restrict__ wg, const u16* __restrict__ wu, const u16* __restrict__ wd, u16* __restrict__ out, unsigned char* ws) {
  __shared__ __attribute__((aligned(16))) float lds_[L_END + (STAMP ? 192 : 0)];
  LAS float* const L = (LAS float*)lds_;
  LAS unsigned* const LU = (LAS unsigned*)lds_;
  LAS char* const LB = (LAS char*)lds_;
  const int tid = threadIdx.x, lane = tid & 63, wv = __builtin_amdgcn_readfirstlane(tid >> 6);
  TS(0);
  LU[L_FLAG + (tid & 7)] = 0u;
  const int xg = blockIdx.x & 7, r = blockIdx.x >> 3;
  const int h = xg * 4 + (r >> 3), b = (r >> 2) & 1, sp = r & 3;
  const bool hasA = r >= 2, hasF = r < 30;
  const int rg = xg * 6 + r / 5, cg = r % 5;
  const int g4 = lane >> 4, j = lane & 15, lb = (lane >> 3) & 1, e = lane & 7, a = lane >> 3;
  asm volatile("" :: "s"(x), "s"(Kc), "s"(Vc), "s"(wq), "s"(wo), "s"(wg), "s"(wu), "s"(wd), "s"(out), "s"(ws));
  const rsrc rW = mkr(ws, WS_END);
  const rsrc rX = mkr(x, 2 * D * 2);
  const v4u msk = {(e >> 1) == 0 ? ((e & 1) ? 0xffff0000u : 0xffffu) : 0u, (e >> 1) == 1 ? ((e & 1) ? 0xffff0000u : 0xffffu) : 0u,
                   (e >> 1) == 2 ? ((e & 1) ? 0xffff0000u : 0xffffu) : 0u, (e >> 1) == 3 ? ((e & 1) ? 0xffff0000u : 0xffffu) : 0u};

#define EPOCH() do { asm volatile("" : "+v"(epoch)); epoch += 1u; } while (0)
  const int ua = hasA ? r - 2 : 0, ks = ua % 5, cp = ua / 5;
  const int row0 = 128 * ks + 16 * a;
  const int ldb = cp < 4 ? NH * DH * 2 : DF * 2;
  const rsrc rA = mkr(cp < 4 ? wq + 192 * (4 * xg + cp) : (cp == 4 ? wg : wu) + 192 * xg, ((ABL & 1) || !hasA || wv < 4) ? 0 : (D - 1) * ldb + 384);
  const int ao = (row0 + 2 * ((wv - 4) & 7)) * ldb + 16 * e;
  v4u ab[6];
#define LDA(i) ab[i] = __builtin_amdgcn_raw_buffer_load_b128(rA, ao + ((i) % 3) * 128, ((i) / 3) * ldb, NT)
  SB();
#pragma unroll
  for (int i = 0; i < W; ++i) { LDA(i); SB(); }
  BAR();
  if (wv >= 4) {
    const int w = wv - 4;
    const size_t kvb = ((size_t)(b * NH + h) * SK + (SK - WIN) + sp * 512) * DH;
    const rsrc rK = mkr(Kc + kvb, 512 * DH * 2), rV = mkr(Vc + kvb, 512 * DH * 2);
    const int kvo = w * 3072 + a * 384 + e * 16;
#define LDK(i) kb[i] = __builtin_amdgcn_raw_buffer_load_b128(rK, kvo + ((i) % 3) * 128, ((i) / 3) * 24576, NT)
#define LDV(i) vb[i] = __builtin_amdgcn_raw_buffer_load_b128(rV, kvo + ((i) % 3) * 128, ((i) / 3) * 24576, NT)
    v4u kb[24], vb[24];
    LAS v4u* const aw = (LAS v4u*)&LU[L_A] + 128 * w + lane;
#pragma unroll
    for (int n = W - 6; n < QCHK; ++n) {
      PACE(n);
      if (n < 0) LDA(n < 0 ? n + 6 : 0); else LDK(n >= 0 ? n : 0);
      SB();
      if (n - W < 0) { const int i = n - W + 6 < 6 ? n - W + 6 : 0; aw[(i % 3) * 1024 + (i / 3) * 64] = ab[i]; }
      if (n - W == -1) lds_post(LU + L_FLAG + 3);
      if (n == 4) TS(1); if (n == 8) TS(2); if (n == 12) TS(3);
    }
    SB();
    TS(4);
    PADTO(3);
    if (__builtin_expect(*(volatile LAS unsigned*)(LU + L_FLAG) < 1u, 0)) lds_wait(LU + L_FLAG, 1);
    MEMF();
    TS(5);
    v4u qv[3];
#pragma unroll
    for (int c = 0; c < 3; ++c) qv[c] = *(LAS v4u*)&LU[L_Q + 32 * c + 4 * e];
    unsigned tag = LU[L_FLAG + 5];
    const rsrc rO = mkr(wo + (size_t)128 * rg * D + 128 * cg, hasF ? 128 * D * 2 : 0), rD = mkr(wd + (size_t)32 * rg * D + 128 * cg, hasF ? 32 * D * 2 : 0);
    int fo = (16 * w + 4 * g4) * 1280 + j * 16, fd = (4 * w + g4) * 1280 + j * 16;
    int pofs = ((w * 2 + lb) * PST + 8 * e + 4 * (g4 & 1)) * 4;
    asm volatile("" : "+v"(fo), "+v"(fd), "+v"(pofs), "+v"(tag));
    v4u fw[5];
    float sc[8];
    f4 o0 = {0.f, 0.f, 0.f, 0.f}, o1 = o0, o2 = o0;
    v4u ap7;
    int f0 = L_A * 4 + j * 16 + (16 * w + 4 * g4) * FRS, f1 = L_A * 4 + j * 16 + (128 + 4 * w + g4) * FRS;
    int foff = lane < 32 ? WS_FP + rg * FS + (cg * 8 + w) * 128 + lane * 4 : WS_END;
    int aofs = L_O * 4 + ((j >> 2) & 1) * 320 + g4 * 16, tro = L_A * 4 + (4 * g4 + (j >> 2)) * FRS + 32 * w + 8 * (j & 3);
    u64 pm = 0xff00ff0000ff00ffull;
#define QK(t) do { float d_ = 0.f; _Pragma("unroll") for (int c = 0; c < 3; ++c) _Pragma("unroll") for (int k = 0; k < 4; ++k) d_ = dot2(kb[3 * (t) + c][k], qv[c][k], d_); \
    sc[t] = rbf(rbf(sum8(d_)) * 0.07216878364870323f) * 1.4426950408889634f; asm volatile("" : "+v"(sc[t])); } while (0)
#define PV(t) do { const v4u ap_ = afrag(sc[t], msk); o0 = mfma(ap_, vb[3 * (t)], o0); o1 = mfma(ap_, vb[3 * (t) + 1], o1); o2 = mfma(ap_, vb[3 * (t) + 2], o2); } while (0)
#define LDF(i) fw[i] = (i) == 4 ? __builtin_amdgcn_raw_buffer_load_b128(rD, fd, 0, NT) : __builtin_amdgcn_raw_buffer_load_b128(rO, fo + (i) * 1280, 0, NT)
#define MX(a_, b_) ((a_) > (b_) ? (a_) : (b_))
#pragma unroll
    for (int n = QCHK; n < 53; ++n) {
      if (n < 47 + W) PACE(n);
      if (n < 24) LDK(n < 24 ? n : 0);
      else if (n < 48) LDV(n >= 24 && n < 48 ? n - 24 : 0);
      else LDF(n >= 48 ? n - 48 : 0);
      SB();
#pragma unroll
      for (int t = 0; t < 8; ++t) if (n == MX(3 * t + 2 + W, QCHK + t)) QK(t);
      if (n == MX(23 + W, QCHK + 7)) {
        TS(6);
        float m = fmaxf(fmaxf(fmaxf(sc[0], sc[1]), fmaxf(sc[2], sc[3])), fmaxf(fmaxf(sc[4], sc[5]), fmaxf(sc[6], sc[7])));
        m = max32(max16(fmaxf(m, dpp<R8>(m))));
        float l = 0.f;
#pragma unroll
        for (int t = 0; t < 8; ++t) { sc[t] = __builtin_amdgcn_exp2f(sc[t] - m); l += sc[t]; }
        l = sum32(sum16(l + dpp<R8>(l)));
        *(LAS f2*)&L[L_ST + 2 * w] = f2{m, l};
        lds_post(LU + L_FLAG + 1);
      }
#pragma unroll
      for (int t = 0; t < 7; ++t) if (n == MX(26 + 3 * t + W, QCHK + 8 + t)) PV(t);
      if (n == LSEN) lds_post(LU + L_FLAG + 2);
      if (n == 40) asm volatile("" : "+v"(f0), "+v"(f1), "+v"(foff), "+v"(aofs), "+v"(tro), "+s"(pm));
      if (n == 47) { TS(7); ap7 = afrag(sc[7], msk); asm volatile("" : "+v"(ap7)); }
    }
    SB();
    o0 = mfma(ap7, vb[21], o0); o1 = mfma(ap7, vb[22], o1); o2 = mfma(ap7, vb[23], o2);
    TS(8);
    if (__builtin_amdgcn_inverse_ballot_w64(pm)) { *(LAS f4*)(LB + pofs) = o0; *(LAS f4*)(LB + pofs + 256) = o1; *(LAS f4*)(LB + pofs + 512) = o2; }
    if (STAMP && TGP >= 2000) { SB(); asm volatile("s_waitcnt vmcnt(0)"); TS(10); }
    BAR();
    if (hasF) {
#pragma unroll
      for (int i = 0; i < 4; ++i) *(LAS v4u*)(LB + f0 + i * FRS) = fw[i];
      *(LAS v4u*)(LB + f1) = fw[4];
      lds_post(LU + L_FLAG + 4);
      for (unsigned s = 0; *(volatile LAS unsigned*)(LU + L_FLAG + 4) < 8u && s < (SPIN_CAP << 4); ++s) {}
      MEMF();
      v4u bf[5];
      const LAS char* const tr = LB + tro;
#pragma unroll
      for (int s = 0; s < 5; ++s) {
        const v2u lo = __builtin_bit_cast(v2u, __builtin_amdgcn_ds_read_tr16_b64_v4i16((LAS s4*)(tr + s * 32 * FRS)));
        const v2u hi = __builtin_bit_cast(v2u, __builtin_amdgcn_ds_read_tr16_b64_v4i16((LAS s4*)(tr + (s * 32 + 16) * FRS)));
        bf[s] = v4u{lo.x, lo.y, hi.x, hi.y};
      }
      f4 acc = {0.f, 0.f, 0.f, 0.f};
      acc = mfma(*(LAS v4u*)(LB + aofs + 256), bf[4], acc);
      asm volatile("" : "+v"(bf[0]), "+v"(bf[1]), "+v"(bf[2]), "+v"(bf[3]), "+v"(acc));
      TS(9);
      BAR();
      v4u af[4];
#pragma unroll
      for (int s = 0; s < 4; ++s) af[s] = *(LAS v4u*)(LB + aofs + s * 64);
      SB();
#pragma unroll
      for (int s = 0; s < 4; ++s) acc = mfma(af[s], bf[s], acc);
      __builtin_amdgcn_raw_buffer_store_b32(tagged(acc.x, tag), rW, foff, 0, SC1);
      TS(11);
      QUIT();
    }
  } else if (wv == 3) {
    const unsigned tstart = (unsigned)__builtin_amdgcn_s_memrealtime() - 60u;
    unsigned epoch = __builtin_amdgcn_raw_buffer_load_b32(rW, WS_E, 0, SC1);
    EPOCH();
    const unsigned tag4 = __builtin_amdgcn_perm(epoch, epoch, 0u);
    LU[L_FLAG + 5] = epoch;
    while ((int)((unsigned)__builtin_amdgcn_s_memrealtime() - tstart) < QT) __builtin_amdgcn_s_sleep(4);
    const int qoff = WS_QP + b * QS + (48 * h + (lane < 48 ? lane : 0)) * 16;
    v4u qg[5] = {};
#pragma nounroll
    for (unsigned spin = 0; spin < ((ABL & 2) ? 0 : SPIN_CAP); ++spin) {
      MEMF();
#pragma unroll
      for (int i = 0; i < 5; ++i) qg[i] = ldws(rW, qoff, i * 2 * QS);
      if (!__any((bad4(qg[0], tag4) | bad4(qg[1], tag4) | bad4(qg[2], tag4) | bad4(qg[3], tag4) | bad4(qg[4], tag4)) != 0u)) break;
      __builtin_amdgcn_s_sleep(8);
    }
    const f4 s = ((un4(qg[0]) + un4(qg[1])) + (un4(qg[2]) + un4(qg[3]))) + un4(qg[4]);
    if (lane < 48) *(LAS v2u*)&LU[L_Q + 32 * g4 + 4 * e + 2 * lb] = v2u{pk(s.x, s.y), pk(s.z, s.w)};
    lds_post(LU + L_FLAG);
    TS(4);
    lds_wait(LU + L_FLAG + 1, 8);
    const f2 ml = *(LAS f2*)&L[L_ST + 2 * e];
    const float M = max8(ml.x), ex = __builtin_amdgcn_exp2f(ml.x - M), Ls = sum8(ml.y * ex);
    L[L_FW + e] = ex / Ls;
    if (!(ABL & 4)) __builtin_amdgcn_raw_buffer_store_b64(v2u{asu(M + __builtin_amdgcn_logf(Ls)), epoch}, rW, lane < 16 ? WS_ST + (h * 8 + b * 4 + sp) * 128 + lane * 8 : WS_END, 0, SC1);
    TS(6);
    const int gq = 32 * rg + (lane & 31), gh = gq / 48;
    const int rho = xg * 10 + r;
    const int oc = (rho & 1) * D + (rho >> 1) * 16 + 4 * (lane & 3) + 2 * (g4 & 1) + (g4 >> 1);
    unsigned xv = __builtin_amdgcn_raw_buffer_load_b16(rX, r < 10 ? oc * 2 : 4096, 0, 0);
    f4 wgt = {0.f, 0.f, 0.f, 0.f};
    if (hasF) {
      lds_wait(LU + L_FLAG + 2, 8);
      const int soff = WS_ST + (gh * 8 + (lane >> 5) * 4 + (lane & 3)) * 128;
      v2u st = {};
#pragma nounroll
      for (unsigned spin = 0; spin < ((ABL & 4) ? 0 : SPIN_CAP); ++spin) {
        MEMF();
        st = __builtin_amdgcn_raw_buffer_load_b64(rW, soff, 0, SC1);
        if (!__any(st.y != epoch)) break;
        __builtin_amdgcn_s_sleep(8);
      }
      float Mg = asf(st.x);
      Mg = fmaxf(Mg, dpp<X1>(Mg)); Mg = fmaxf(Mg, dpp<X2>(Mg));
      float ws_ = __builtin_amdgcn_exp2f(asf(st.x) - Mg);
      ws_ /= sum4(ws_);
      wgt = f4{dpp<0x00>(ws_), dpp<0x55>(ws_), dpp<0xAA>(ws_), dpp<0xFF>(ws_)};
    }
    int ooff = WS_OP + (lane >> 5) * OS + gq * 16;
    int aofs = L_O * 4 + (lane >> 5) * 320 + ((lane >> 3) & 3) * 64 + (lane & 3) * 16 + ((lane >> 2) & 1) * 8;
    int roff = WS_FP + (lane >> 2) * FS + rho * 64 + (lane & 3) * 16;
    int oofs = j < 4 && (OUTM == 0 || lane < 32) ? oc * 2 : 4096;
    if (OPRE) { unsigned pre = __builtin_amdgcn_raw_buffer_load_b32(mkr(out, 2 * D * 2), r < 10 && lane == 0 ? oc * 2 : 4096, 0, 0); asm volatile("" :: "v"(pre)); }
    float xf = bflo(xv);
    const rsrc rOut = mkr(out, 2 * D * 2);
    asm volatile("" : "+v"(ooff), "+v"(aofs), "+v"(roff), "+v"(xf), "+v"(wgt), "+v"(oofs));
    PADTO(2);
    BAR();
    TS(8);
    if (hasF) {
      v4u op[4] = {};
      __builtin_amdgcn_s_sleep(PDELAY);
      if (!(ABL & 8)) sweep4(rW, ooff, 2 * OS, 4 * OS, 6 * OS, tag4, op[0], op[1], op[2], op[3]);
      TS(10);
      const f4 o = (un4(op[0]) * wgt.x + un4(op[1]) * wgt.y) + (un4(op[2]) * wgt.z + un4(op[3]) * wgt.w);
      *(LAS v2u*)(LB + aofs) = v2u{pk(o.x, o.y), pk(o.z, o.w)};
      BAR();
      if (r < 10) {
        v4u fp[3] = {};
        TS(12);
        if (DEP) asm volatile("s_waitcnt vmcnt(0)" : "+{v[64:67]}"(op[0]), "+{v[68:71]}"(op[1]), "+{v[72:75]}"(op[2]), "+{v[76:79]}"(op[3]));
        __builtin_amdgcn_s_sleep(RDELAY);
        if (!(ABL & 8)) sweep3(rW, roff, 16 * FS, 32 * FS, tag4, fp[0], fp[1], fp[2]);
        TS(13);
        float t = rowsum4((un4(fp[0]) + un4(fp[1])) + un4(fp[2]));
        t += dpp<R4>(t); t += dpp<R8>(t);
        { t += xf;
          const auto u = __builtin_amdgcn_permlane32_swap(asu(t), asu(t), false, false);
          __builtin_amdgcn_raw_buffer_store_b32(pk(asf(u[0]), asf(u[1])), rOut, oofs, 0, OAUX); }
        if (rho == 0 && !NOE) __builtin_amdgcn_raw_buffer_store_b32(epoch, rW, lane == 0 ? WS_E : WS_END, 0, EAUX);
        if (DEP3) asm volatile("" :: "{v[64:67]}"(fp[0]), "{v[68:71]}"(fp[1]), "{v[72:75]}"(fp[2]));
        TS(14);
      }
    }
    QUIT();
    asm volatile("s_branch 1f\n\t.p2align 12\n1:");
  } else {
    const int sv = wv;
    unsigned epoch = __builtin_amdgcn_raw_buffer_load_b32(rW, WS_E, 0, SC1);
    const int jj = e | (g4 << 3);
    const v4u xs0 = __builtin_amdgcn_raw_buffer_load_b128(rX, lb * 1280 + jj * 16, 0, 0);
    const v4u xs1 = __builtin_amdgcn_raw_buffer_load_b128(rX, lb * 1280 + 512 + jj * 16, 0, 0);
    const v2u xs2 = __builtin_amdgcn_raw_buffer_load_b64(rX, lb * 1280 + 1024 + jj * 8, 0, 0);
    SB();
    EPOCH();
    float v[20];
#pragma unroll
    for (int i = 0; i < 4; ++i) { v[2 * i] = bflo(xs0[i]); v[2 * i + 1] = bfhi(xs0[i]); v[8 + 2 * i] = bflo(xs1[i]); v[9 + 2 * i] = bfhi(xs1[i]); }
    v[16] = bflo(xs2[0]); v[17] = bfhi(xs2[0]); v[18] = bflo(xs2[1]); v[19] = bfhi(xs2[1]);
    float s = 0.f;
#pragma unroll
    for (int i = 0; i < 20; ++i) s += v[i];
    s = sum32(sum16(sum8(s)));
    const float mean = s * (1.f / D);
    float q = 0.f;
#pragma unroll
    for (int i = 0; i < 20; ++i) { const float d = v[i] - mean; q = fmaf(d, d, q); }
    q = sum32(sum16(sum8(q)));
    const float mb = rbf(mean), rs = 1.f / rbf(sqrtf(rbf(rbf(q * (1.f / D)) + 1e-5f)));
#pragma unroll
    for (int i = 0; i < 5; ++i) {
      v4u o;
#pragma unroll
      for (int k = 0; k < 4; ++k) { const float t = rbf(v[4 * i + k] - mb) * rs; o[k] = pk(t, t); }
      *(LAS v4u*)&LU[L_XN + lb * 640 + (i < 4 ? (i >> 1) * 256 + 8 * jj + 4 * (i & 1) : 512 + 4 * jj)] = o;
    }
    MEMF();
    TS(1);
    lds_wait(LU + L_FLAG + 3, 8);
    const LAS v4u* const wa = (const LAS v4u*)&LU[L_A] + 1024 * sv + lane;
    f4 a0 = {0.f, 0.f, 0.f, 0.f}, a1 = a0;
#pragma nounroll
    for (int t4 = 0; t4 < 4; ++t4) {
      const v4u d0 = *(LAS v4u*)&LU[L_XN + row0 + 4 * t4], d1 = *(LAS v4u*)&LU[L_XN + 640 + row0 + 4 * t4];
#pragma unroll
      for (int k = 0; k < 4; ++k) {
        const v4u wt = wa[(4 * t4 + k) * 64];
        a0 = mfma(v4u{d0[k] & msk.x, d0[k] & msk.y, d0[k] & msk.z, d0[k] & msk.w}, wt, a0);
        a1 = mfma(v4u{d1[k] & msk.x, d1[k] & msk.y, d1[k] & msk.z, d1[k] & msk.w}, wt, a1);
      }
    }
    TS(2);
    v4u y;
#pragma unroll
    for (int i = 0; i < 4; ++i) { const auto s2 = __builtin_amdgcn_permlane32_swap(asu(a0[i]), asu(a1[i]), false, false); y[i] = tagged(asf(s2[0]) + dpp<R8>(asf(s2[1])), epoch); }
    const int abase = cp < 4 ? WS_QP + ks * 2 * QS + 48 * (4 * xg + cp) * 16 : WS_GU + (ks * 4 + cp - 4) * GS + 48 * xg * 16, abs_ = cp < 4 ? QS : 2 * GS;
    __builtin_amdgcn_raw_buffer_store_b128(y, rW, lb == 0 && hasA && !(ABL & 2) ? abase + (lane >> 5) * abs_ + (16 * sv + 8 * (g4 & 1) + e) * 16 : WS_END, 0, SC1);
    TS(3);
    if (sv == 1 && hasF) {
      const unsigned tag4 = __builtin_amdgcn_perm(epoch, epoch, 0u);
      lds_wait(LU + L_FLAG + 1, 8);
      const int goff = WS_GU + ((lane >> 5) * 12 + lb * 2 + (g4 & 1)) * GS + (16 * (rg >> 1) + 8 * (e >> 2) + 4 * (rg & 1) + (e & 3)) * 16, g2 = lane < 32 ? goff + 8 * GS : goff;
      v4u gp[3] = {};
#pragma nounroll
      for (unsigned spin = 0; spin < ((ABL & 2) ? 0 : SPIN_CAP); ++spin) {
        MEMF();
        gp[0] = ldws(rW, goff); gp[1] = ldws(rW, goff, 4 * GS); gp[2] = ldws(rW, g2);
        if (!__any((bad4(gp[0], tag4) | bad4(gp[1], tag4) | bad4(gp[2], tag4)) != 0u)) break;
        __builtin_amdgcn_s_sleep(8);
      }
      f4 s4_ = un4(gp[0]) + un4(gp[1]);
      if (lane < 32) s4_ += un4(gp[2]);
      const auto sw = __builtin_amdgcn_permlane16_swap(asu(swadd32(s4_.x, s4_.y)), asu(swadd32(s4_.z, s4_.w)), false, false);
      const float g = rbf(asf(sw[0])), u = rbf(asf(sw[1]));
      const float z = 0.7978845608028654f * (g + 0.044715f * g * g * g);
      const float th = 1.f - 2.f / (1.f + __expf(2.f * z));
      const int fq = 2 * (e & 3) + (e >> 2);
      *(LAS u16*)(LB + L_O * 4 + lb * 320 + 256 + (fq & 3) * 16 + (fq >> 2) * 8 + (2 * (g4 & 1) + (g4 >> 1)) * 2) = (u16)pk(rbf(0.5f * g * (1.f + th)) * u, 0.f);
      TS(5);
    }
    int ooff = WS_OP + (sp * 2 + b) * OS + (192 * h + 64 * sv + 4 * j + 2 * (g4 & 1) + (g4 >> 1)) * 4;
    int mofs = (L_PART + g4 * 4 * PST + 64 * sv + 4 * j) * 4, wofs = (L_FW + 2 * g4) * 4;
    asm volatile("" : "+v"(ooff), "+v"(mofs), "+v"(wofs));
    PADTO(1);
    BAR();
    TS(8);
    const f2 fa = *(LAS f2*)(LB + wofs);
    f4 t[4];
#pragma unroll
    for (int i = 0; i < 4; ++i) t[i] = *(LAS f4*)(LB + mofs + i * PST * 4);
    SB();
    __builtin_amdgcn_raw_buffer_store_b32(tagged(rowsum4((t[0] + t[1]) * fa.x + (t[2] + t[3]) * fa.y), epoch), rW, ooff, 0, SC1);
    TS(9);
    QUIT();
  }
  FINISH();
}
}

extern "C" void kernel_launch(void* const* d_in, const int* in_sizes, int n_in, void* d_out, int out_size, void* d_ws, size_t ws_size, hipStream_t stream) {
  fused<<<256, 768, 0, stream>>>((const u16*)d_in[0], (const u16*)d_in[1], (const u16*)d_in[2], (const u16*)d_in[3], (const u16*)d_in[4],
                                 (const u16*)d_in[6], (const u16*)d_in[7], (const u16*)d_in[8], (u16*)d_out, (unsigned char*)d_ws);
}
```

```cpp
#include <hip/hip_runtime.h>
#include <hip/hip_bf16.h>
#include <stdint.h>

namespace {
typedef unsigned v4u __attribute__((ext_vector_type(4)));
typedef unsigned v2u __attribute__((ext_vector_type(2)));
typedef float f4 __attribute__((ext_vector_type(4)));
typedef float f2 __attribute__((ext_vector_type(2)));
typedef __bf16 bf8v __attribute__((ext_vector_type(8)));
typedef __bf16 bf2v __attribute__((ext_vector_type(2)));
typedef unsigned short u16;
#define LAS __attribute__((address_space(3)))
#define CAS __attribute__((address_space(4)))
#define T_Q 320
#define T_S 280
#define T_I 120
#define T_B 308
#define N_W 4

constexpr int D = 640, SC1 = 16;
constexpr unsigned SPIN = 1u << 18, STEP = 0x01010101u;
constexpr int QS = 6144 * 4 + 768, GS = 1536 * 4 + 128, FS = 1280 * 4 + 128;
constexpr int WS_E = 0;
constexpr int WS_QP = 4096;
constexpr int WS_GU = WS_QP + 10 * QS;
constexpr int WS_ST = WS_GU + 20 * GS;
constexpr int WS_FP = WS_ST + 256 * 128;
constexpr int WS_AC = WS_FP + 48 * FS;
constexpr int WS_END = WS_AC + 160 * 128;
constexpr int PST = 196;
constexpr int L_Q = 16 * PST;
constexpr int L_O = L_Q + 96;
constexpr int L_ST = L_O + 160;
constexpr int L_FW = L_ST + 16;
constexpr int L_FLAG = L_FW + 8;
constexpr int F_Q = L_FLAG, F_ST = L_FLAG + 1, F_POS = L_FLAG + 2, F_A = L_FLAG + 3, F_W = L_FLAG + 4, F_TAG = L_FLAG + 5, F_T1 = L_FLAG + 6, F_TB = L_FLAG + 7;
constexpr int L_XN = L_FLAG + 8;
constexpr int L_XR = L_XN + 1280;
constexpr int L_TR = L_XR + 640;
constexpr int L_A = L_TR + 320;
constexpr int L_F = L_A + 48 * 256;
constexpr int L_END = L_F + 40 * 1056 / 4;
static_assert(L_FLAG == 3416 && L_XR == 4704 && L_A == 5664 && L_F == 17952 && L_END == 28512 && L_O == 3232 && L_ST == 3392 && WS_FP == 415744 && WS_AC == 667648 && WS_END == 688128 && FS == 5248, "constants of the assembly block");

__device__ __forceinline__ float asf(unsigned u) { return __builtin_bit_cast(float, u); }
__device__ __forceinline__ unsigned asu(float f) { return __builtin_bit_cast(unsigned, f); }
__device__ __forceinline__ float bflo(unsigned u) { return asf(u << 16); }
__device__ __forceinline__ float bfhi(unsigned u) { return asf(u & 0xffff0000u); }
__device__ __forceinline__ unsigned pk(float lo, float hi) { bf2v r = {(__bf16)lo, (__bf16)hi}; return __builtin_bit_cast(unsigned, r); }
__device__ __forceinline__ float rbf(float f) { return (float)(__bf16)f; }
template <int CTRL> __device__ __forceinline__ float dpp(float x) { return asf(__builtin_amdgcn_mov_dpp(asu(x), CTRL, 0xf, 0xf, true)); }
constexpr int X1 = 0xB1, X2 = 0x4E, HM = 0x141, R8 = 0x128;
__device__ __forceinline__ float swadd16(float x, float y) { auto s = __builtin_amdgcn_permlane16_swap(asu(x), asu(y), false, false); return asf(s[0]) + asf(s[1]); }
__device__ __forceinline__ float swadd32(float x, float y) { auto s = __builtin_amdgcn_permlane32_swap(asu(x), asu(y), false, false); return asf(s[0]) + asf(s[1]); }
__device__ __forceinline__ float sum4(float x) { x += dpp<X1>(x); x += dpp<X2>(x); return x; }
__device__ __forceinline__ float sum8(float x) { x = sum4(x); x += dpp<HM>(x); return x; }
__device__ __forceinline__ float max4(float x) { x = fmaxf(x, dpp<X1>(x)); return fmaxf(x, dpp<X2>(x)); }
__device__ __forceinline__ float sum32of(float x) { x = sum8(x); x = swadd16(x, x); return swadd32(x, x); }
__device__ __forceinline__ f4 mfma(v4u a, v4u b, f4 c) { return __builtin_amdgcn_mfma_f32_16x16x32_bf16(__builtin_bit_cast(bf8v, a), __builtin_bit_cast(bf8v, b), c, 0, 0, 0); }
typedef __amdgpu_buffer_rsrc_t rsrc;
__device__ __forceinline__ rsrc mkr(const void* p, int bytes) { return __builtin_amdgcn_make_buffer_rsrc((void*)p, 0, bytes, 0x00020000); }
#define SB() __builtin_amdgcn_sched_barrier(0)
#define MEMF() asm volatile("" ::: "memory")
#define BAR() do { SB(); asm volatile("s_waitcnt lgkmcnt(0)\n\ts_barrier" ::: "memory"); SB(); } while (0)
#define QUIT() asm volatile("s_endpgm" ::: "memory")
__device__ __forceinline__ unsigned now() { return (unsigned)__builtin_amdgcn_s_memrealtime(); }
__device__ __forceinline__ void until(unsigned t) { while (t - now() < 1024u) __builtin_amdgcn_s_sleep(4); }
__device__ __forceinline__ unsigned tagged(float v, unsigned tag) { return __builtin_amdgcn_perm(asu(v), tag, 0x07060500u); }
template <int N, int SLEEP> __device__ __forceinline__ void gather(rsrc r, const int (&off)[N], const int (&soff)[N], unsigned tag, f4 (&v)[N]) {
  const v4u t8 = {tag & 0xffu, (tag >> 8) & 0xffu, (tag >> 16) & 0xffu, tag >> 24};
#pragma nounroll
  for (unsigned spin = 0; spin < SPIN; ++spin) {
    MEMF();
    v4u p[N];
#pragma unroll
    for (int i = 0; i < N; ++i) p[i] = __builtin_amdgcn_raw_buffer_load_b128(r, off[i], soff[i], SC1);
    unsigned bad = 0;
#pragma unroll
    for (int i = 0; i < N; ++i) { p[i] ^= t8; asm("" : "+v"(p[i])); bad |= p[i].x | p[i].y; bad |= p[i].z | p[i].w; v[i] = __builtin_bit_cast(f4, p[i]); }
    if (!__any((bad & 0xffu) != 0u)) break;
    if (SLEEP) __builtin_amdgcn_s_sleep(SLEEP);
  }
}
template <int SLEEP> __device__ __forceinline__ void lds_wait(LAS unsigned* f, unsigned want) {
  for (unsigned s = 0; *(volatile LAS unsigned*)f < want && s < (SPIN << 3); ++s) __builtin_amdgcn_s_sleep(SLEEP);
  MEMF();
}
__device__ __forceinline__ void lds_post(LAS unsigned* f) {
  asm volatile("s_mov_b64 exec, 1\n\tds_add_u32 %0, %1\n\ts_mov_b64 exec, -1" :: "v"((unsigned)(size_t)f), "v"(1u) : "memory");
}

struct Args { const u16 *x, *K, *V, *wq, *wo, *wg, *wu, *wd; u16* out; unsigned char* ws; };

__global__ void __launch_bounds__(768, 3) __attribute__((aligned(4096))) fused(const u16* x_, const u16* K_, const u16* V_, const u16* wq_, const u16* wo_,
    const u16* wg_, const u16* wu_, const u16* wd_, u16* out_, unsigned char* ws_) {
  __shared__ __attribute__((aligned(16))) float lds_[L_END + 192];
  const CAS Args* ka = (const CAS Args*)__builtin_amdgcn_kernarg_segment_ptr();
  const int tid = threadIdx.x;
  asm volatile(
    "v_readfirstlane_b32 s80, v0\n"
    "s_cmp_ge_u32 s80, 0x200\n"
    "s_cbranch_scc1 Lskip_%=\n"
    "s_load_dwordx16 s[36:51], %[ka], 0x0\n"
    "s_and_b32 s81, %[bid], 7\n"
    "v_and_b32 v133, 7, v0\n"
    "s_lshr_b32 s82, %[bid], 3\n"
    "s_lshr_b32 s80, s80, 6\n"
    "s_max_u32 s84, s82, 2\n"
    "s_sub_u32 s84, s84, 2\n"
    "s_mul_i32 s85, s84, 52\n"
    "s_lshr_b32 s85, s85, 8\n"
    "s_mul_i32 s86, s85, 5\n"
    "s_sub_u32 s84, s84, s86\n"
    "s_lshl_b32 s84, s84, 6\n"
    "s_add_u32 s84, s84, s80\n"
    "s_lshl_b32 s84, s84, 1\n"
    "s_lshl2_add_u32 s86, s81, s85\n"
    "s_movk_i32 s87, 0xc00\n"
    "s_cmp_lt_u32 s85, 4\n"
    "s_cselect_b32 s83, 0x3000, s87\n"
    "s_cselect_b32 s86, s86, s81\n"
    "s_mul_i32 s86, s86, 384\n"
    "s_mul_i32 s84, s84, s83\n"
    "s_add_u32 s84, s84, s86\n"
    "s_mul_i32 s58, s83, 113\n"
    "s_add_u32 s58, s58, 384\n"
    "s_cmp_lt_u32 s82, 2\n"
    "s_cselect_b32 s58, 0, s58\n"
    "s_mov_b32 s59, 0x20000\n"
    "s_lshl_b32 s86, s83, 4\n"
    "v_bfe_u32 v134, v0, 3, 3\n"
    "v_lshlrev_b32 v138, 4, v133\n"
    "v_mad_u32_u24 v3, v134, s86, v138\n"
    "s_lshl_b32 s88, s80, 11\n"
    "s_add_u32 m0, s88, 22656\n"
    "s_waitcnt lgkmcnt(0)\n"
    "s_cmp_eq_u32 s85, 4\n"
    "s_cselect_b64 s[56:57], s[46:47], s[48:49]\n"
    "s_cmp_lt_u32 s85, 4\n"
    "s_cselect_b64 s[56:57], s[42:43], s[56:57]\n"
    "s_add_u32 s56, s56, s84\n"
    "s_addc_u32 s57, s57, 0\n"
    "s_pack_ll_b32_b16 s57, s57, 0\n"
    "buffer_load_dwordx4 v3, s[56:59], 0 offen offset:0 nt lds\n"
    "s_addk_i32 m0, 16256\n"
    "s_nop 0\n"
    "buffer_load_dwordx4 v3, s[56:59], 0 offen offset:128 nt lds\n"
    "s_addk_i32 m0, 16256\n"
    "s_load_dwordx2 s[52:53], %[ka], 0x48\n"
    "s_load_dwordx2 s[96:97], %[ka], 0x40\n"
    "s_lshr_b32 s90, %[bid], 6\n"
    "s_lshl2_add_u32 s90, s81, s90\n"
    "s_and_b32 s86, %[bid], 32\n"
    "s_add_u32 s90, s90, s86\n"
    "s_mul_i32 s90, s90, 0x300000\n"
    "s_bfe_u32 s86, %[bid], 0x20003\n"
    "s_mul_i32 s86, s86, 3072\n"
    "s_add_u32 s90, s90, s86\n"
    "s_add_u32 s90, s90, 0x240000\n"
    "s_sub_u32 s62, 0xc0000, s86\n"
    "s_mov_b32 s63, s59\n"
    "s_bitcmp1_b32 %[bid], 6\n"
    "s_cselect_b32 s34, -1, 1\n"
    "s_cselect_b32 s3, 0xa8000, 0\n"
    "s_mul_i32 s34, s34, 0x18000\n"
    "s_mov_b32 s89, s3\n"
    "s_mul_i32 s86, s80, 0x3000\n"
    "v_mul_u32_u24 v1, 0x180, v134\n"
    "v_add_u32 v1, s86, v1\n"
    "v_lshl_add_u32 v1, v133, 4, v1\n"
    "s_add_u32 s60, s38, s90\n"
    "s_addc_u32 s61, s39, 0\n"
    "s_pack_ll_b32_b16 s61, s61, 0\n"
    "s_add_u32 s64, s40, s90\n"
    "s_addc_u32 s65, s41, 0\n"
    "s_pack_ll_b32_b16 s65, s65, 0\n"
    "s_mov_b64 s[66:67], s[62:63]\n"
    "s_mov_b32 s71, s59\n"
    "s_mov_b32 s75, s59\n"
    "s_mov_b32 s79, s59\n"
    "s_mov_b32 s99, s59\n"
    "s_mul_i32 s84, s82, 52\n"
    "s_lshr_b32 s84, s84, 8\n"
    "s_mul_i32 s85, s84, 5\n"
    "s_sub_u32 s85, s82, s85\n"
    "s_mul_i32 s86, s81, 6\n"
    "s_add_u32 s84, s84, s86\n"
    "s_lshl3_add_u32 s41, s85, s80\n"
    "s_lshl_b32 s40, s41, 9\n"
    "s_add_u32 s40, s40, 667648\n"
    "s_lshl_b32 s90, s41, 7\n"
    "s_lshl_b32 s41, s41, 5\n"
    "s_mul_i32 s86, s84, 5248\n"
    "s_add_u32 s90, s90, s86\n"
    "s_add_u32 s90, s90, 415744\n"
    "s_lshl_b32 s85, s85, 8\n"
    "s_mul_i32 s86, s80, 0x500\n"
    "s_add_u32 s85, s85, s86\n"
    "s_mul_i32 s86, s84, 0xa000\n"
    "s_add_u32 s87, s86, s85\n"
    "s_add_u32 s72, s50, s87\n"
    "s_addc_u32 s73, s51, 0\n"
    "s_lshl2_add_u32 s87, s86, s85\n"
    "s_add_u32 s68, s44, s87\n"
    "s_addc_u32 s69, s45, 0\n"
    "s_cmp_lt_u32 s82, 30\n"
    "s_cselect_b32 s70, 153856, 0\n"
    "s_cselect_b32 s74, 30976, 0\n"
    "s_waitcnt lgkmcnt(0)\n"
    "s_mov_b64 s[76:77], s[52:53]\n"
    "s_mov_b32 s78, 688128\n"
    "s_movk_i32 s98, 0xa00\n"
    "s_pack_ll_b32_b16 s69, s69, 0\n"
    "s_pack_ll_b32_b16 s73, s73, 0\n"
    "s_pack_ll_b32_b16 s77, s77, 0\n"
    "s_pack_ll_b32_b16 s97, s97, 0\n"
    "s_mov_b32 s42, 0x3d93cd3a\n"
    "s_mov_b32 s93, 0x3fb8aa3b\n"
    "s_mov_b32 s94, 0x07060500\n"
    "s_mul_i32 s88, s80, 1056\n"
    "s_add_u32 s88, s88, 71808\n"
    "s_mov_b32 s91, 0x200000\n"
    "s_movk_i32 s35, 460\n"
    "s_movk_i32 s95, 7648\n"
    "v_mov_b32 v135, 1\n"
    "v_mov_b32 v136, 13664\n"
    "v_cmp_gt_u32 vcc, 4, v133\n"
    "v_mov_b32 v138, 0xffff\n"
    "v_mov_b32 v139, 0\n"
    "v_lshlrev_b32 v28, 4, v133\n"
    "v_lshlrev_b64 v[138:139], v28, v[138:139]\n"
    "v_cndmask_b32 v4, 0, v138, vcc\n"
    "v_cndmask_b32 v5, 0, v139, vcc\n"
    "v_xor_b32 v6, v4, v138\n"
    "v_xor_b32 v7, v5, v139\n"
    "v_bfe_u32 v138, v0, 4, 2\n"
    "v_and_b32 v139, 15, v0\n"
    "v_lshlrev_b32 v131, 2, v139\n"
    "v_lshl_add_u32 v131, v138, 6, v131\n"
    "v_lshlrev_b32 v137, 7, v134\n"
    "v_lshl_add_u32 v137, v133, 3, v137\n"
    "v_mul_u32_u24 v140, 0x500, v138\n"
    "v_lshl_add_u32 v140, v139, 1, v140\n"
    "v_mul_u32_u24 v2, 0x2800, v138\n"
    "v_lshl_add_u32 v2, v139, 4, v2\n"
    "v_bfe_u32 v129, v0, 2, 1\n"
    "v_mul_u32_u24 v129, 0x140, v129\n"
    "v_lshl_add_u32 v129, v138, 4, v129\n"
    "v_bfe_u32 v130, v0, 2, 3\n"
    "v_mul_u32_u24 v130, 0x420, v130\n"
    "v_bfe_u32 v28, v0, 5, 1\n"
    "v_lshl_add_u32 v130, v28, 8, v130\n"
    "v_and_b32 v29, 3, v0\n"
    "v_lshl_add_u32 v130, v29, 3, v130\n"
    "s_lshl_b32 s84, s80, 5\n"
    "s_add_u32 s84, s84, 71808\n"
    "v_add_u32 v130, s84, v130\n"
    "v_bfe_u32 v29, v0, 3, 1\n"
    "v_bfe_u32 v30, v0, 4, 1\n"
    "v_lshlrev_b32 v128, 5, v133\n"
    "v_lshl_add_u32 v128, v30, 4, v128\n"
    "s_mul_i32 s84, s80, 1568\n"
    "v_mul_u32_u24 v30, 784, v29\n"
    "v_add_u32 v30, s84, v30\n"
    "v_lshlrev_b32 v31, 8, v28\n"
    "v_add_u32 v31, 21376, v31\n"
    "v_cmp_eq_u32 vcc, v28, v29\n"
    "s_nop 1\n"
    "v_cndmask_b32 v30, v31, v30, vcc\n"
    "v_add_u32 v128, v128, v30\n"
    "v_lshlrev_b32 v133, 4, v133\n"
    "s_branch Lgov_%=\n"
    "Lgate_%=:\n"
    "ds_read_b32 v134, v136 offset:28\n"
    "s_memrealtime s[84:85]\n"
    "s_waitcnt lgkmcnt(0)\n"
    "v_readfirstlane_b32 s92, v134\n"
    "s_sub_u32 s84, s84, s92\n"
    "s_lshl_b32 s84, s84, 4\n"
    "s_cmp_lt_u32 s84, s95\n"
    "s_cbranch_scc1 Lgate_%=\n"
    "s_sub_u32 s84, s84, s95\n"
    "s_cmpk_lt_u32 s84, 1200\n"
    "s_cbranch_scc0 Lgw_%=\n"
    "s_waitcnt vmcnt(2)\n"
    "Lgw_%=:\n"
    "s_add_u32 s95, s95, s35\n"
    "s_waitcnt vmcnt(3)\n"
    "s_setpc_b64 s[54:55]\n"
    "Lgov_%=:\n"
    "s_barrier\n"
    "s_waitcnt vmcnt(2)\n"
    "buffer_load_dwordx4 v3, s[56:59], 0 offen offset:256 nt lds\n"
    "s_addk_i32 m0, -31488\n"
    "s_waitcnt vmcnt(2)\n"
    "buffer_load_dwordx4 v3, s[56:59], s83 offen offset:0 nt lds\n"
    "s_addk_i32 m0, 16256\n"
    "s_memrealtime s[48:49]\n"
    "s_waitcnt vmcnt(2)\n"
    "buffer_load_dwordx4 v3, s[56:59], s83 offen offset:128 nt lds\n"
    "s_addk_i32 m0, 16256\n"
    "s_waitcnt vmcnt(2)\n"
    "buffer_load_dwordx4 v3, s[56:59], s83 offen offset:256 nt lds\n"
    "s_waitcnt vmcnt(2)\n"
    "buffer_load_dwordx4 v[32:35], v1, s[60:63], s89 offen offset:0 nt\n"
    "s_waitcnt vmcnt(2)\n"
    "buffer_load_dwordx4 v[36:39], v1, s[60:63], s89 offen offset:128 nt\n"
    "s_waitcnt vmcnt(2)\n"
    "buffer_load_dwordx4 v[40:43], v1, s[60:63], s89 offen offset:256 nt\n"
    "s_add_u32 s89, s89, s34\n"
    "s_mov_b64 exec, 1\n"
    "ds_add_u32 v136, v135 offset:12\n"
    "s_mov_b64 exec, -1\n"
    "s_waitcnt vmcnt(2)\n"
    "buffer_load_dwordx4 v[44:47], v1, s[60:63], s89 offen offset:0 nt\n"
    "s_waitcnt vmcnt(2)\n"
    "buffer_load_dwordx4 v[48:51], v1, s[60:63], s89 offen offset:128 nt\n"
    "s_waitcnt vmcnt(2)\n"
    "buffer_load_dwordx4 v[52:55], v1, s[60:63], s89 offen offset:256 nt\n"
    "s_add_u32 s89, s89, s34\n"
    "s_waitcnt vmcnt(2)\n"
    "buffer_load_dwordx4 v[56:59], v1, s[60:63], s89 offen offset:0 nt\n"
    "s_memrealtime s[84:85]\n"
    "s_waitcnt lgkmcnt(0)\n"
    "v_mov_b32 v134, s84\n"
    "v_mov_b32 v138, s48\n"
    "s_mov_b64 exec, 1\n"
    "ds_max_u32 v136, v134 offset:28\n"
    "ds_min_u32 v136, v138 offset:24\n"
    "ds_add_u32 v136, v135 offset:8\n"
    "s_mov_b64 exec, -1\n"
    "s_waitcnt vmcnt(2)\n"
    "buffer_load_dwordx4 v[60:63], v1, s[60:63], s89 offen offset:128 nt\n"
    "s_waitcnt vmcnt(2)\n"
    "buffer_load_dwordx4 v[64:67], v1, s[60:63], s89 offen offset:256 nt\n"
    "s_add_u32 s89, s89, s34\n"
    "s_waitcnt vmcnt(2)\n"
    "buffer_load_dwordx4 v[68:71], v1, s[60:63], s89 offen offset:0 nt\n"
    "s_waitcnt vmcnt(2)\n"
    "buffer_load_dwordx4 v[72:75], v1, s[60:63], s89 offen offset:128 nt\n"
    "s_waitcnt vmcnt(2)\n"
    "buffer_load_dwordx4 v[76:79], v1, s[60:63], s89 offen offset:256 nt\n"
    "s_add_u32 s89, s89, s34\n"
    "s_waitcnt vmcnt(2)\n"
    "buffer_load_dwordx4 v[80:83], v1, s[60:63], s89 offen offset:0 nt\n"
    "s_waitcnt vmcnt(2)\n"
    "buffer_load_dwordx4 v[84:87], v1, s[60:63], s89 offen offset:128 nt\n"
    "s_waitcnt vmcnt(2)\n"
    "buffer_load_dwordx4 v[88:91], v1, s[60:63], s89 offen offset:256 nt\n"
    "s_add_u32 s89, s89, s34\n"
    "s_waitcnt vmcnt(2)\n"
    "buffer_load_dwordx4 v[92:95], v1, s[60:63], s89 offen offset:0 nt\n"
    "s_waitcnt vmcnt(2)\n"
    "buffer_load_dwordx4 v[96:99], v1, s[60:63], s89 offen offset:128 nt\n"
    "s_waitcnt vmcnt(2)\n"
    "buffer_load_dwordx4 v[100:103], v1, s[60:63], s89 offen offset:256 nt\n"
    "s_add_u32 s89, s89, s34\n"
    "s_waitcnt vmcnt(2)\n"
    "buffer_load_dwordx4 v[104:107], v1, s[60:63], s89 offen offset:0 nt\n"
    "s_waitcnt vmcnt(2)\n"
    "buffer_load_dwordx4 v[108:111], v1, s[60:63], s89 offen offset:128 nt\n"
    "s_waitcnt vmcnt(2)\n"
    "buffer_load_dwordx4 v[112:115], v1, s[60:63], s89 offen offset:256 nt\n"
    "s_add_u32 s89, s89, s34\n"
    "s_waitcnt vmcnt(2)\n"
    "buffer_load_dwordx4 v[116:119], v1, s[60:63], s89 offen offset:0 nt\n"
    "s_waitcnt vmcnt(2)\n"
    "buffer_load_dwordx4 v[120:123], v1, s[60:63], s89 offen offset:128 nt\n"
    "s_waitcnt vmcnt(2)\n"
    "buffer_load_dwordx4 v[124:127], v1, s[60:63], s89 offen offset:256 nt\n"
    "s_mov_b32 s89, s3\n"
    "s_call_b64 s[54:55], Lgate_%=\n"
    "buffer_load_dwordx4 v[144:147], v1, s[64:67], s89 offen offset:0 nt\n"
    "s_call_b64 s[54:55], Lgate_%=\n"
    "buffer_load_dwordx4 v[148:151], v1, s[64:67], s89 offen offset:128 nt\n"
    "Lq0_%=:\n"
    "ds_read_b32 v138, v136 offset:0\n"
    "s_sub_u32 s91, s91, 1\n"
    "s_cbranch_scc1 Lq1_%=\n"
    "s_waitcnt lgkmcnt(0)\n"
    "v_cmp_gt_u32 vcc, 1, v138\n"
    "s_cbranch_vccz Lq1_%=\n"
    "s_sleep 2\n"
    "s_branch Lq0_%=\n"
    "Lq1_%=:\n"
    "ds_read_b128 v[8:11], v133 offset:12544\n"
    "ds_read_b128 v[12:15], v133 offset:12672\n"
    "ds_read_b128 v[16:19], v133 offset:12800\n"
    "ds_read_b32 v132, v136 offset:20\n"
    "s_call_b64 s[54:55], Lgate_%=\n"
    "buffer_load_dwordx4 v[152:155], v1, s[64:67], s89 offen offset:256 nt\n"
    "s_add_u32 s89, s89, s34\n"
    "s_waitcnt lgkmcnt(0)\n"
    "v_mov_b32 v20, 0\n"
    "v_mov_b32 v21, 0\n"
    "v_dot2c_f32_bf16 v20, v32, v8\n"
    "v_dot2c_f32_bf16 v21, v44, v8\n"
    "v_dot2c_f32_bf16 v20, v33, v9\n"
    "v_dot2c_f32_bf16 v21, v45, v9\n"
    "v_dot2c_f32_bf16 v20, v34, v10\n"
    "v_dot2c_f32_bf16 v21, v46, v10\n"
    "v_dot2c_f32_bf16 v20, v35, v11\n"
    "v_dot2c_f32_bf16 v21, v47, v11\n"
    "v_dot2c_f32_bf16 v20, v36, v12\n"
    "v_dot2c_f32_bf16 v21, v48, v12\n"
    "v_dot2c_f32_bf16 v20, v37, v13\n"
    "v_dot2c_f32_bf16 v21, v49, v13\n"
    "v_dot2c_f32_bf16 v20, v38, v14\n"
    "v_dot2c_f32_bf16 v21, v50, v14\n"
    "v_dot2c_f32_bf16 v20, v39, v15\n"
    "v_dot2c_f32_bf16 v21, v51, v15\n"
    "v_dot2c_f32_bf16 v20, v40, v16\n"
    "v_dot2c_f32_bf16 v21, v52, v16\n"
    "v_dot2c_f32_bf16 v20, v41, v17\n"
    "v_dot2c_f32_bf16 v21, v53, v17\n"
    "v_dot2c_f32_bf16 v20, v42, v18\n"
    "v_dot2c_f32_bf16 v21, v54, v18\n"
    "v_dot2c_f32_bf16 v20, v43, v19\n"
    "v_dot2c_f32_bf16 v21, v55, v19\n"
    "s_nop 1\n"
    "v_add_f32_dpp v20, v20, v20 quad_perm:[1,0,3,2] row_mask:0xf bank_mask:0xf bound_ctrl:1\n"
    "v_add_f32_dpp v21, v21, v21 quad_perm:[1,0,3,2] row_mask:0xf bank_mask:0xf bound_ctrl:1\n"
    "s_nop 0\n"
    "v_add_f32_dpp v20, v20, v20 quad_perm:[2,3,0,1] row_mask:0xf bank_mask:0xf bound_ctrl:1\n"
    "v_add_f32_dpp v21, v21, v21 quad_perm:[2,3,0,1] row_mask:0xf bank_mask:0xf bound_ctrl:1\n"
    "s_nop 0\n"
    "v_add_f32_dpp v20, v20, v20 row_half_mirror row_mask:0xf bank_mask:0xf bound_ctrl:1\n"
    "v_add_f32_dpp v21, v21, v21 row_half_mirror row_mask:0xf bank_mask:0xf bound_ctrl:1\n"
    "v_cvt_pk_bf16_f32 v20, 0, v20\n"
    "v_mul_f32 v20, s42, v20\n"
    "v_cvt_pk_bf16_f32 v20, 0, v20\n"
    "v_cvt_pk_bf16_f32 v21, 0, v21\n"
    "v_mul_f32 v21, s42, v21\n"
    "v_cvt_pk_bf16_f32 v21, 0, v21\n"
    "s_call_b64 s[54:55], Lgate_%=\n"
    "buffer_load_dwordx4 v[32:35], v1, s[64:67], s89 offen offset:0 nt\n"
    "v_mov_b32 v22, 0\n"
    "v_mov_b32 v23, 0\n"
    "v_dot2c_f32_bf16 v22, v56, v8\n"
    "v_dot2c_f32_bf16 v23, v68, v8\n"
    "v_dot2c_f32_bf16 v22, v57, v9\n"
    "v_dot2c_f32_bf16 v23, v69, v9\n"
    "v_dot2c_f32_bf16 v22, v58, v10\n"
    "v_dot2c_f32_bf16 v23, v70, v10\n"
    "v_dot2c_f32_bf16 v22, v59, v11\n"
    "v_dot2c_f32_bf16 v23, v71, v11\n"
    "v_dot2c_f32_bf16 v22, v60, v12\n"
    "v_dot2c_f32_bf16 v23, v72, v12\n"
    "v_dot2c_f32_bf16 v22, v61, v13\n"
    "v_dot2c_f32_bf16 v23, v73, v13\n"
    "v_dot2c_f32_bf16 v22, v62, v14\n"
    "v_dot2c_f32_bf16 v23, v74, v14\n"
    "v_dot2c_f32_bf16 v22, v63, v15\n"
    "v_dot2c_f32_bf16 v23, v75, v15\n"
    "v_dot2c_f32_bf16 v22, v64, v16\n"
    "v_dot2c_f32_bf16 v23, v76, v16\n"
    "v_dot2c_f32_bf16 v22, v65, v17\n"
    "v_dot2c_f32_bf16 v23, v77, v17\n"
    "v_dot2c_f32_bf16 v22, v66, v18\n"
    "v_dot2c_f32_bf16 v23, v78, v18\n"
    "v_dot2c_f32_bf16 v22, v67, v19\n"
    "v_dot2c_f32_bf16 v23, v79, v19\n"
    "s_nop 1\n"
    "v_add_f32_dpp v22, v22, v22 quad_perm:[1,0,3,2] row_mask:0xf bank_mask:0xf bound_ctrl:1\n"
    "v_add_f32_dpp v23, v23, v23 quad_perm:[1,0,3,2] row_mask:0xf bank_mask:0xf bound_ctrl:1\n"
    "s_nop 0\n"
    "v_add_f32_dpp v22, v22, v22 quad_perm:[2,3,0,1] row_mask:0xf bank_mask:0xf bound_ctrl:1\n"
    "v_add_f32_dpp v23, v23, v23 quad_perm:[2,3,0,1] row_mask:0xf bank_mask:0xf bound_ctrl:1\n"
    "s_nop 0\n"
    "v_add_f32_dpp v22, v22, v22 row_half_mirror row_mask:0xf bank_mask:0xf bound_ctrl:1\n"
    "v_add_f32_dpp v23, v23, v23 row_half_mirror row_mask:0xf bank_mask:0xf bound_ctrl:1\n"
    "v_cvt_pk_bf16_f32 v22, 0, v22\n"
    "v_mul_f32 v22, s42, v22\n"
    "v_cvt_pk_bf16_f32 v22, 0, v22\n"
    "v_cvt_pk_bf16_f32 v23, 0, v23\n"
    "v_mul_f32 v23, s42, v23\n"
    "v_cvt_pk_bf16_f32 v23, 0, v23\n"
    "s_call_b64 s[54:55], Lgate_%=\n"
    "buffer_load_dwordx4 v[36:39], v1, s[64:67], s89 offen offset:128 nt\n"
    "v_mov_b32 v24, 0\n"
    "v_mov_b32 v25, 0\n"
    "v_dot2c_f32_bf16 v24, v80, v8\n"
    "v_dot2c_f32_bf16 v25, v92, v8\n"
    "v_dot2c_f32_bf16 v24, v81, v9\n"
    "v_dot2c_f32_bf16 v25, v93, v9\n"
    "v_dot2c_f32_bf16 v24, v82, v10\n"
    "v_dot2c_f32_bf16 v25, v94, v10\n"
    "v_dot2c_f32_bf16 v24, v83, v11\n"
    "v_dot2c_f32_bf16 v25, v95, v11\n"
    "v_dot2c_f32_bf16 v24, v84, v12\n"
    "v_dot2c_f32_bf16 v25, v96, v12\n"
    "v_dot2c_f32_bf16 v24, v85, v13\n"
    "v_dot2c_f32_bf16 v25, v97, v13\n"
    "v_dot2c_f32_bf16 v24, v86, v14\n"
    "v_dot2c_f32_bf16 v25, v98, v14\n"
    "v_dot2c_f32_bf16 v24, v87, v15\n"
    "v_dot2c_f32_bf16 v25, v99, v15\n"
    "v_dot2c_f32_bf16 v24, v88, v16\n"
    "v_dot2c_f32_bf16 v25, v100, v16\n"
    "v_dot2c_f32_bf16 v24, v89, v17\n"
    "v_dot2c_f32_bf16 v25, v101, v17\n"
    "v_dot2c_f32_bf16 v24, v90, v18\n"
    "v_dot2c_f32_bf16 v25, v102, v18\n"
    "v_dot2c_f32_bf16 v24, v91, v19\n"
    "v_dot2c_f32_bf16 v25, v103, v19\n"
    "s_nop 1\n"
    "v_add_f32_dpp v24, v24, v24 quad_perm:[1,0,3,2] row_mask:0xf bank_mask:0xf bound_ctrl:1\n"
    "v_add_f32_dpp v25, v25, v25 quad_perm:[1,0,3,2] row_mask:0xf bank_mask:0xf bound_ctrl:1\n"
    "s_nop 0\n"
    "v_add_f32_dpp v24, v24, v24 quad_perm:[2,3,0,1] row_mask:0xf bank_mask:0xf bound_ctrl:1\n"
    "v_add_f32_dpp v25, v25, v25 quad_perm:[2,3,0,1] row_mask:0xf bank_mask:0xf bound_ctrl:1\n"
    "s_nop 0\n"
    "v_add_f32_dpp v24, v24, v24 row_half_mirror row_mask:0xf bank_mask:0xf bound_ctrl:1\n"
    "v_add_f32_dpp v25, v25, v25 row_half_mirror row_mask:0xf bank_mask:0xf bound_ctrl:1\n"
    "v_cvt_pk_bf16_f32 v24, 0, v24\n"
    "v_mul_f32 v24, s42, v24\n"
    "v_cvt_pk_bf16_f32 v24, 0, v24\n"
    "v_cvt_pk_bf16_f32 v25, 0, v25\n"
    "v_mul_f32 v25, s42, v25\n"
    "v_cvt_pk_bf16_f32 v25, 0, v25\n"
    "s_call_b64 s[54:55], Lgate_%=\n"
    "buffer_load_dwordx4 v[40:43], v1, s[64:67], s89 offen offset:256 nt\n"
    "s_add_u32 s89, s89, s34\n"
    "v_mov_b32 v26, 0\n"
    "v_mov_b32 v27, 0\n"
    "v_dot2c_f32_bf16 v26, v104, v8\n"
    "v_dot2c_f32_bf16 v27, v116, v8\n"
    "v_dot2c_f32_bf16 v26, v105, v9\n"
    "v_dot2c_f32_bf16 v27, v117, v9\n"
    "v_dot2c_f32_bf16 v26, v106, v10\n"
    "v_dot2c_f32_bf16 v27, v118, v10\n"
    "v_dot2c_f32_bf16 v26, v107, v11\n"
    "v_dot2c_f32_bf16 v27, v119, v11\n"
    "v_dot2c_f32_bf16 v26, v108, v12\n"
    "v_dot2c_f32_bf16 v27, v120, v12\n"
    "v_dot2c_f32_bf16 v26, v109, v13\n"
    "v_dot2c_f32_bf16 v27, v121, v13\n"
    "v_dot2c_f32_bf16 v26, v110, v14\n"
    "v_dot2c_f32_bf16 v27, v122, v14\n"
    "v_dot2c_f32_bf16 v26, v111, v15\n"
    "v_dot2c_f32_bf16 v27, v123, v15\n"
    "v_dot2c_f32_bf16 v26, v112, v16\n"
    "v_dot2c_f32_bf16 v27, v124, v16\n"
    "v_dot2c_f32_bf16 v26, v113, v17\n"
    "v_dot2c_f32_bf16 v27, v125, v17\n"
    "v_dot2c_f32_bf16 v26, v114, v18\n"
    "v_dot2c_f32_bf16 v27, v126, v18\n"
    "v_dot2c_f32_bf16 v26, v115, v19\n"
    "v_dot2c_f32_bf16 v27, v127, v19\n"
    "s_nop 1\n"
    "v_add_f32_dpp v26, v26, v26 quad_perm:[1,0,3,2] row_mask:0xf bank_mask:0xf bound_ctrl:1\n"
    "v_add_f32_dpp v27, v27, v27 quad_perm:[1,0,3,2] row_mask:0xf bank_mask:0xf bound_ctrl:1\n"
    "s_nop 0\n"
    "v_add_f32_dpp v26, v26, v26 quad_perm:[2,3,0,1] row_mask:0xf bank_mask:0xf bound_ctrl:1\n"
    "v_add_f32_dpp v27, v27, v27 quad_perm:[2,3,0,1] row_mask:0xf bank_mask:0xf bound_ctrl:1\n"
    "s_nop 0\n"
    "v_add_f32_dpp v26, v26, v26 row_half_mirror row_mask:0xf bank_mask:0xf bound_ctrl:1\n"
    "v_add_f32_dpp v27, v27, v27 row_half_mirror row_mask:0xf bank_mask:0xf bound_ctrl:1\n"
    "v_cvt_pk_bf16_f32 v26, 0, v26\n"
    "v_mul_f32 v26, s42, v26\n"
    "v_cvt_pk_bf16_f32 v26, 0, v26\n"
    "v_cvt_pk_bf16_f32 v27, 0, v27\n"
    "v_mul_f32 v27, s42, v27\n"
    "v_cvt_pk_bf16_f32 v27, 0, v27\n"
    "v_max3_f32 v138, v20, v21, v22\n"
    "v_max3_f32 v28, v23, v24, v25\n"
    "v_max3_f32 v138, v138, v26, v27\n"
    "v_max_f32 v138, v138, v28\n"
    "s_nop 1\n"
    "v_max_f32_dpp v138, v138, v138 row_ror:8 row_mask:0xf bank_mask:0xf bound_ctrl:1\n"
    "v_mov_b32 v28, v138\n"
    "s_nop 1\n"
    "v_permlane16_swap_b32 v138, v28\n"
    "v_max_f32 v138, v138, v28\n"
    "v_mov_b32 v28, v138\n"
    "s_nop 1\n"
    "v_permlane32_swap_b32 v138, v28\n"
    "v_max_f32 v138, v138, v28\n"
    "v_mul_f32 v138, s93, v138\n"
    "v_fma_f32 v20, v20, s93, -v138\n"
    "v_fma_f32 v21, v21, s93, -v138\n"
    "v_fma_f32 v22, v22, s93, -v138\n"
    "v_fma_f32 v23, v23, s93, -v138\n"
    "v_fma_f32 v24, v24, s93, -v138\n"
    "v_fma_f32 v25, v25, s93, -v138\n"
    "v_fma_f32 v26, v26, s93, -v138\n"
    "v_fma_f32 v27, v27, s93, -v138\n"
    "v_exp_f32 v20, v20\n"
    "v_exp_f32 v21, v21\n"
    "v_exp_f32 v22, v22\n"
    "v_exp_f32 v23, v23\n"
    "v_exp_f32 v24, v24\n"
    "v_exp_f32 v25, v25\n"
    "v_exp_f32 v26, v26\n"
    "v_exp_f32 v27, v27\n"
    "v_add_f32 v139, v20, v21\n"
    "v_add_f32 v139, v139, v22\n"
    "v_add_f32 v139, v139, v23\n"
    "v_add_f32 v139, v139, v24\n"
    "v_add_f32 v139, v139, v25\n"
    "v_add_f32 v139, v139, v26\n"
    "v_add_f32 v139, v139, v27\n"
    "s_nop 1\n"
    "v_add_f32_dpp v139, v139, v139 row_ror:8 row_mask:0xf bank_mask:0xf bound_ctrl:1\n"
    "v_mov_b32 v28, v139\n"
    "s_nop 1\n"
    "v_permlane16_swap_b32 v139, v28\n"
    "v_add_f32 v139, v139, v28\n"
    "v_mov_b32 v28, v139\n"
    "s_nop 1\n"
    "v_permlane32_swap_b32 v139, v28\n"
    "v_add_f32 v139, v139, v28\n"
    "s_lshl_b32 s84, s80, 3\n"
    "v_mov_b32 v28, s84\n"
    "s_mov_b64 exec, 1\n"
    "ds_write_b64 v28, v[138:139] offset:13568\n"
    "ds_add_u32 v136, v135 offset:4\n"
    "s_mov_b64 exec, -1\n"
    "s_call_b64 s[54:55], Lgate_%=\n"
    "buffer_load_dwordx4 v[44:47], v1, s[64:67], s89 offen offset:0 nt\n"
    "v_cvt_pk_bf16_f32 v31, v20, v20\n"
    "v_and_b32 v28, v4, v31\n"
    "v_and_b32 v29, v5, v31\n"
    "v_and_b32 v30, v6, v31\n"
    "v_and_b32 v31, v7, v31\n"
    "s_nop 1\n"
    "v_mfma_f32_16x16x32_bf16 v[8:11], v[28:31], v[144:147], 0\n"
    "v_mfma_f32_16x16x32_bf16 v[12:15], v[28:31], v[148:151], 0\n"
    "v_mfma_f32_16x16x32_bf16 v[16:19], v[28:31], v[152:155], 0\n"
    "s_call_b64 s[54:55], Lgate_%=\n"
    "buffer_load_dwordx4 v[48:51], v1, s[64:67], s89 offen offset:128 nt\n"
    "s_call_b64 s[54:55], Lgate_%=\n"
    "buffer_load_dwordx4 v[52:55], v1, s[64:67], s89 offen offset:256 nt\n"
    "s_add_u32 s89, s89, s34\n"
    "s_call_b64 s[54:55], Lgate_%=\n"
    "buffer_load_dwordx4 v[56:59], v1, s[64:67], s89 offen offset:0 nt\n"
    "v_cvt_pk_bf16_f32 v31, v21, v21\n"
    "v_and_b32 v28, v4, v31\n"
    "v_and_b32 v29, v5, v31\n"
    "v_and_b32 v30, v6, v31\n"
    "v_and_b32 v31, v7, v31\n"
    "s_nop 1\n"
    "v_mfma_f32_16x16x32_bf16 v[8:11], v[28:31], v[32:35], v[8:11]\n"
    "v_mfma_f32_16x16x32_bf16 v[12:15], v[28:31], v[36:39], v[12:15]\n"
    "v_mfma_f32_16x16x32_bf16 v[16:19], v[28:31], v[40:43], v[16:19]\n"
    "s_call_b64 s[54:55], Lgate_%=\n"
    "buffer_load_dwordx4 v[60:63], v1, s[64:67], s89 offen offset:128 nt\n"
    "s_call_b64 s[54:55], Lgate_%=\n"
    "buffer_load_dwordx4 v[64:67], v1, s[64:67], s89 offen offset:256 nt\n"
    "s_add_u32 s89, s89, s34\n"
    "s_call_b64 s[54:55], Lgate_%=\n"
    "buffer_load_dwordx4 v[68:71], v1, s[64:67], s89 offen offset:0 nt\n"
    "v_cvt_pk_bf16_f32 v31, v22, v22\n"
    "v_and_b32 v28, v4, v31\n"
    "v_and_b32 v29, v5, v31\n"
    "v_and_b32 v30, v6, v31\n"
    "v_and_b32 v31, v7, v31\n"
    "s_nop 1\n"
    "v_mfma_f32_16x16x32_bf16 v[8:11], v[28:31], v[44:47], v[8:11]\n"
    "v_mfma_f32_16x16x32_bf16 v[12:15], v[28:31], v[48:51], v[12:15]\n"
    "v_mfma_f32_16x16x32_bf16 v[16:19], v[28:31], v[52:55], v[16:19]\n"
    "s_call_b64 s[54:55], Lgate_%=\n"
    "buffer_load_dwordx4 v[72:75], v1, s[64:67], s89 offen offset:128 nt\n"
    "s_call_b64 s[54:55], Lgate_%=\n"
    "buffer_load_dwordx4 v[76:79], v1, s[64:67], s89 offen offset:256 nt\n"
    "s_add_u32 s89, s89, s34\n"
    "s_call_b64 s[54:55], Lgate_%=\n"
    "buffer_load_dwordx4 v[80:83], v1, s[64:67], s89 offen offset:0 nt\n"
    "v_cvt_pk_bf16_f32 v31, v23, v23\n"
    "v_and_b32 v28, v4, v31\n"
    "v_and_b32 v29, v5, v31\n"
    "v_and_b32 v30, v6, v31\n"
    "v_and_b32 v31, v7, v31\n"
    "s_nop 1\n"
    "v_mfma_f32_16x16x32_bf16 v[8:11], v[28:31], v[56:59], v[8:11]\n"
    "v_mfma_f32_16x16x32_bf16 v[12:15], v[28:31], v[60:63], v[12:15]\n"
    "v_mfma_f32_16x16x32_bf16 v[16:19], v[28:31], v[64:67], v[16:19]\n"
    "s_call_b64 s[54:55], Lgate_%=\n"
    "buffer_load_dwordx4 v[84:87], v1, s[64:67], s89 offen offset:128 nt\n"
    "s_call_b64 s[54:55], Lgate_%=\n"
    "buffer_load_dwordx4 v[88:91], v1, s[64:67], s89 offen offset:256 nt\n"
    "s_add_u32 s89, s89, s34\n"
    "s_call_b64 s[54:55], Lgate_%=\n"
    "buffer_load_dwordx4 v[92:95], v1, s[64:67], s89 offen offset:0 nt\n"
    "v_cvt_pk_bf16_f32 v31, v24, v24\n"
    "v_and_b32 v28, v4, v31\n"
    "v_and_b32 v29, v5, v31\n"
    "v_and_b32 v30, v6, v31\n"
    "v_and_b32 v31, v7, v31\n"
    "s_nop 1\n"
    "v_mfma_f32_16x16x32_bf16 v[8:11], v[28:31], v[68:71], v[8:11]\n"
    "v_mfma_f32_16x16x32_bf16 v[12:15], v[28:31], v[72:75], v[12:15]\n"
    "v_mfma_f32_16x16x32_bf16 v[16:19], v[28:31], v[76:79], v[16:19]\n"
    "s_call_b64 s[54:55], Lgate_%=\n"
    "buffer_load_dwordx4 v[96:99], v1, s[64:67], s89 offen offset:128 nt\n"
    "s_call_b64 s[54:55], Lgate_%=\n"
    "buffer_load_dwordx4 v[100:103], v1, s[64:67], s89 offen offset:256 nt\n"
    "s_add_u32 s89, s89, s34\n"
    "s_call_b64 s[54:55], Lgate_%=\n"
    "buffer_load_dwordx4 v[104:107], v1, s[64:67], s89 offen offset:0 nt\n"
    "v_cvt_pk_bf16_f32 v31, v25, v25\n"
    "v_and_b32 v28, v4, v31\n"
    "v_and_b32 v29, v5, v31\n"
    "v_and_b32 v30, v6, v31\n"
    "v_and_b32 v31, v7, v31\n"
    "s_nop 1\n"
    "v_mfma_f32_16x16x32_bf16 v[8:11], v[28:31], v[80:83], v[8:11]\n"
    "v_mfma_f32_16x16x32_bf16 v[12:15], v[28:31], v[84:87], v[12:15]\n"
    "v_mfma_f32_16x16x32_bf16 v[16:19], v[28:31], v[88:91], v[16:19]\n"
    "s_call_b64 s[54:55], Lgate_%=\n"
    "buffer_load_dwordx4 v[108:111], v1, s[64:67], s89 offen offset:128 nt\n"
    "s_call_b64 s[54:55], Lgate_%=\n"
    "buffer_load_dwordx4 v[112:115], v1, s[64:67], s89 offen offset:256 nt\n"
    "s_mov_b32 s89, 0\n"
    "v_cvt_pk_bf16_f32 v31, v26, v26\n"
    "v_and_b32 v28, v4, v31\n"
    "v_and_b32 v29, v5, v31\n"
    "v_and_b32 v30, v6, v31\n"
    "v_and_b32 v31, v7, v31\n"
    "s_nop 0\n"
    "s_waitcnt vmcnt(5)\n"
    "v_mfma_f32_16x16x32_bf16 v[8:11], v[28:31], v[92:95], v[8:11]\n"
    "s_waitcnt vmcnt(4)\n"
    "v_mfma_f32_16x16x32_bf16 v[12:15], v[28:31], v[96:99], v[12:15]\n"
    "s_waitcnt vmcnt(3)\n"
    "v_mfma_f32_16x16x32_bf16 v[16:19], v[28:31], v[100:103], v[16:19]\n"
    "s_call_b64 s[54:55], Lgate_%=\n"
    "s_mov_b32 m0, s88\n"
    "s_nop 0\n"
    "buffer_load_dwordx4 v2, s[68:71], s89 offen nt lds\n"
    "v_cvt_pk_bf16_f32 v31, v27, v27\n"
    "v_and_b32 v28, v4, v31\n"
    "v_and_b32 v29, v5, v31\n"
    "v_and_b32 v30, v6, v31\n"
    "v_and_b32 v31, v7, v31\n"
    "s_nop 0\n"
    "s_waitcnt vmcnt(3)\n"
    "v_mfma_f32_16x16x32_bf16 v[8:11], v[28:31], v[104:107], v[8:11]\n"
    "s_addk_i32 m0, 8448\n"
    "s_add_u32 s89, s89, 0xa000\n"
    "buffer_load_dwordx4 v2, s[68:71], s89 offen nt lds\n"
    "s_waitcnt vmcnt(3)\n"
    "v_mfma_f32_16x16x32_bf16 v[12:15], v[28:31], v[108:111], v[12:15]\n"
    "s_addk_i32 m0, 8448\n"
    "s_add_u32 s89, s89, 0xa000\n"
    "buffer_load_dwordx4 v2, s[68:71], s89 offen nt lds\n"
    "s_addk_i32 m0, 8448\n"
    "s_add_u32 s89, s89, 0xa000\n"
    "buffer_load_dwordx4 v2, s[68:71], s89 offen nt lds\n"
    "s_addk_i32 m0, 8448\n"
    "s_nop 0\n"
    "buffer_load_dwordx4 v2, s[72:75], 0 offen nt lds\n"
    "ds_write_b128 v128, v[8:11]\n"
    "ds_write_b128 v128, v[12:15] offset:256\n"
    "s_waitcnt vmcnt(5)\n"
    "v_mfma_f32_16x16x32_bf16 v[16:19], v[28:31], v[112:115], v[16:19]\n"
    "s_nop 7\n"
    "ds_write_b128 v128, v[16:19] offset:512\n"
    "s_waitcnt lgkmcnt(0)\n"
    "s_barrier\n"
    "s_cmp_lt_u32 s82, 30\n"
    "s_cbranch_scc0 Lend_%=\n"
    "v_add_u32 v141, s41, v140\n"
    "ds_read_u16 v141, v141 offset:18816\n"
    "ds_read_b128 v[28:31], v129 offset:13184\n"
    "v_lshlrev_b32 v88, 3, v0\n"
    "v_lshrrev_b32 v88, v88, v132\n"
    "v_mov_b32 v86, 0x41\n"
    "v_mov_b32 v87, 0x1001\n"
    "s_mov_b32 s43, 0x42000000\n"
    "s_mov_b32 s46, 0x39800000\n"
    "s_waitcnt vmcnt(0)\n"
    "s_mov_b64 exec, 1\n"
    "ds_add_u32 v136, v135 offset:16\n"
    "s_mov_b64 exec, -1\n"
    "Lw0_%=:\n"
    "ds_read_b32 v138, v136 offset:16\n"
    "s_sub_u32 s91, s91, 1\n"
    "s_cbranch_scc1 Lw1_%=\n"
    "s_waitcnt lgkmcnt(0)\n"
    "v_cmp_gt_u32 vcc, 8, v138\n"
    "s_cbranch_vccnz Lw0_%=\n"
    "Lw1_%=:\n"
    "ds_read_b64_tr_b16 v[48:49], v130 offset:33792\n"
    "ds_read_b64_tr_b16 v[50:51], v130 offset:34304\n"
    "ds_read_b64_tr_b16 v[32:33], v130 offset:0\n"
    "ds_read_b64_tr_b16 v[34:35], v130 offset:512\n"
    "ds_read_b64_tr_b16 v[36:37], v130 offset:8448\n"
    "ds_read_b64_tr_b16 v[38:39], v130 offset:8960\n"
    "ds_read_b64_tr_b16 v[40:41], v130 offset:16896\n"
    "ds_read_b64_tr_b16 v[42:43], v130 offset:17408\n"
    "ds_read_b64_tr_b16 v[44:45], v130 offset:25344\n"
    "ds_read_b64_tr_b16 v[46:47], v130 offset:25856\n"
    "v_lshlrev_b32 v141, 16, v141\n"
    "s_waitcnt lgkmcnt(8)\n"
    "v_mfma_f32_16x16x32_bf16 v[8:11], v[28:31], v[48:51], 0\n"
    "s_waitcnt lgkmcnt(0)\n"
    "s_barrier\n"
    "ds_read_b128 v[64:67], v129 offset:12928\n"
    "ds_read_b128 v[68:71], v129 offset:12992\n"
    "ds_read_b128 v[72:75], v129 offset:13056\n"
    "ds_read_b128 v[76:79], v129 offset:13120\n"
    "s_waitcnt lgkmcnt(3)\n"
    "v_mfma_f32_16x16x32_bf16 v[8:11], v[64:67], v[32:35], v[8:11]\n"
    "s_waitcnt lgkmcnt(2)\n"
    "v_mfma_f32_16x16x32_bf16 v[8:11], v[68:71], v[36:39], v[8:11]\n"
    "s_waitcnt lgkmcnt(1)\n"
    "v_mfma_f32_16x16x32_bf16 v[8:11], v[72:75], v[40:43], v[8:11]\n"
    "s_waitcnt lgkmcnt(0)\n"
    "v_mfma_f32_16x16x32_bf16 v[8:11], v[76:79], v[44:47], v[8:11]\n"
    "s_nop 6\n"
    "s_mov_b32 exec_hi, 0\n"
    "v_ldexp_f32 v84, v8, 20\n"
    "v_cmp_nlt_f32 s[84:85], |v8|, s43\n"
    "v_cmp_ge_f32 vcc, |v8|, s46\n"
    "v_fract_f32 v80, v84\n"
    "v_cvt_flr_i32_f32 v81, v84\n"
    "v_ldexp_f32 v80, v80, 14\n"
    "v_cndmask_b32 v82, 1, v86, vcc\n"
    "v_cvt_u32_f32 v80, v80\n"
    "v_cndmask_b32 v83, v81, 0, s[84:85]\n"
    "v_lshl_or_b32 v80, v80, 18, v82\n"
    "v_cndmask_b32 v82, v80, v87, s[84:85]\n"
    "v_mov_b64 v[80:81], v[82:83]\n"
    "buffer_atomic_add_x2 v[82:83], v137, s[76:79], s40 offen sc0\n"
    "v_perm_b32 v8, v8, v88, s94\n"
    "buffer_store_dword v8, v131, s[76:79], s90 offen sc1\n"
    "s_waitcnt vmcnt(1)\n"
    "v_lshl_add_u64 v[80:81], v[80:81], 0, v[82:83]\n"
    "v_and_b32 v82, 63, v80\n"
    "v_bfe_u32 v83, v80, 6, 12\n"
    "v_lshrrev_b32 v80, 18, v80\n"
    "v_cmp_eq_u32 vcc, 48, v82\n"
    "v_cvt_f32_i32 v81, v81\n"
    "v_cvt_f32_u32 v80, v80\n"
    "v_add_u32 v83, -1, v83\n"
    "s_mov_b64 exec, vcc\n"
    "s_cbranch_execz Lend_%=\n"
    "v_cmp_lt_u32 vcc, 62, v83\n"
    "v_fmac_f32 v81, 0x38800000, v80\n"
    "v_mul_f32 v81, 0x35800000, v81\n"
    "s_cbranch_vccnz Lslow_%=\n"
    "Lfin_%=:\n"
    "v_add_f32 v141, v141, v81\n"
    "v_cvt_pk_bf16_f32 v141, v141, v141\n"
    "buffer_store_short v141, v140, s[96:99], s41 offen sc1\n"
    "s_cmp_lg_u32 s41, 0\n"
    "s_cbranch_scc1 Lend_%=\n"
    "s_and_b64 exec, exec, 1\n"
    "s_cbranch_execz Lend_%=\n"
    "v_add_u32 v132, 0x01010101, v132\n"
    "buffer_store_dword v132, off, s[76:79], 0 sc1\n"
    "Lend_%=:\n"
    "s_endpgm\n"
    "Lslow_%=:\n"
    "s_mul_i32 s86, s81, 6\n"
    "s_mul_i32 s87, s82, 52\n"
    "s_lshr_b32 s87, s87, 8\n"
    "s_add_u32 s86, s86, s87\n"
    "s_mul_i32 s86, s86, 5248\n"
    "s_sub_u32 s90, s90, s86\n"
    "s_mov_b32 s84, exec_lo\n"
    "s_mov_b32 s85, vcc_lo\n"
    "s_mov_b32 exec_lo, -1\n"
    "s_mov_b32 s86, 4\n"
    "s_mov_b32 s91, 0x40000\n"
    "v_mov_b32 v84, 0\n"
    "v_and_b32 v83, 0xff, v88\n"
    "Lsl1_%=:\n"
    "s_mov_b32 s87, s90\n"
    "buffer_load_dword v32, v131, s[76:79], s87 offen sc1\n"
    "s_addk_i32 s87, 5248\n"
    "buffer_load_dword v33, v131, s[76:79], s87 offen sc1\n"
    "s_addk_i32 s87, 5248\n"
    "buffer_load_dword v34, v131, s[76:79], s87 offen sc1\n"
    "s_addk_i32 s87, 5248\n"
    "buffer_load_dword v35, v131, s[76:79], s87 offen sc1\n"
    "s_addk_i32 s87, 5248\n"
    "buffer_load_dword v36, v131, s[76:79], s87 offen sc1\n"
    "s_addk_i32 s87, 5248\n"
    "buffer_load_dword v37, v131, s[76:79], s87 offen sc1\n"
    "s_addk_i32 s87, 5248\n"
    "buffer_load_dword v38, v131, s[76:79], s87 offen sc1\n"
    "s_addk_i32 s87, 5248\n"
    "buffer_load_dword v39, v131, s[76:79], s87 offen sc1\n"
    "s_addk_i32 s87, 5248\n"
    "buffer_load_dword v40, v131, s[76:79], s87 offen sc1\n"
    "s_addk_i32 s87, 5248\n"
    "buffer_load_dword v41, v131, s[76:79], s87 offen sc1\n"
    "s_addk_i32 s87, 5248\n"
    "buffer_load_dword v42, v131, s[76:79], s87 offen sc1\n"
    "s_addk_i32 s87, 5248\n"
    "buffer_load_dword v43, v131, s[76:79], s87 offen sc1\n"
    "s_addk_i32 s87, 5248\n"
    "s_sub_u32 s91, s91, 1\n"
    "s_cbranch_scc1 Lend_%=\n"
    "s_waitcnt vmcnt(0)\n"
    "v_mov_b32 v82, 0\n"
    "v_xor_b32 v32, v32, v83\n"
    "v_or_b32 v82, v82, v32\n"
    "v_xor_b32 v33, v33, v83\n"
    "v_or_b32 v82, v82, v33\n"
    "v_xor_b32 v34, v34, v83\n"
    "v_or_b32 v82, v82, v34\n"
    "v_xor_b32 v35, v35, v83\n"
    "v_or_b32 v82, v82, v35\n"
    "v_xor_b32 v36, v36, v83\n"
    "v_or_b32 v82, v82, v36\n"
    "v_xor_b32 v37, v37, v83\n"
    "v_or_b32 v82, v82, v37\n"
    "v_xor_b32 v38, v38, v83\n"
    "v_or_b32 v82, v82, v38\n"
    "v_xor_b32 v39, v39, v83\n"
    "v_or_b32 v82, v82, v39\n"
    "v_xor_b32 v40, v40, v83\n"
    "v_or_b32 v82, v82, v40\n"
    "v_xor_b32 v41, v41, v83\n"
    "v_or_b32 v82, v82, v41\n"
    "v_xor_b32 v42, v42, v83\n"
    "v_or_b32 v82, v82, v42\n"
    "v_xor_b32 v43, v43, v83\n"
    "v_or_b32 v82, v82, v43\n"
    "v_and_b32 v82, 0xff, v82\n"
    "v_cmp_ne_u32 vcc, 0, v82\n"
    "s_cbranch_vccnz Lsl1_%=\n"
    "v_add_f32 v84, v84, v32\n"
    "v_add_f32 v84, v84, v33\n"
    "v_add_f32 v84, v84, v34\n"
    "v_add_f32 v84, v84, v35\n"
    "v_add_f32 v84, v84, v36\n"
    "v_add_f32 v84, v84, v37\n"
    "v_add_f32 v84, v84, v38\n"
    "v_add_f32 v84, v84, v39\n"
    "v_add_f32 v84, v84, v40\n"
    "v_add_f32 v84, v84, v41\n"
    "v_add_f32 v84, v84, v42\n"
    "v_add_f32 v84, v84, v43\n"
    "s_mov_b32 s90, s87\n"
    "s_sub_u32 s86, s86, 1\n"
    "s_cmp_lg_u32 s86, 0\n"
    "s_cbranch_scc1 Lsl1_%=\n"
    "s_mov_b32 exec_lo, s85\n"
    "v_mov_b32 v81, v84\n"
    "s_mov_b32 exec_lo, s84\n"
    "s_branch Lfin_%=\n"
    ".p2align 8\n"
    "Lskip_%=:\n"
    "s_setprio 1\n"
    : [ka] "+{s[0:1]}"(ka) : [bid] "{s2}"((int)blockIdx.x), "{v0}"(tid) : "s80", "s99", "v155", "scc", "vcc", "memory");
  LAS float* const L = (LAS float*)lds_;
  LAS unsigned* const LU = (LAS unsigned*)lds_;
  LAS char* const LB = (LAS char*)lds_;
  const int lane = tid & 63, wv = __builtin_amdgcn_readfirstlane(tid >> 6) & 3;
  if (wv == 2) LU[L_FLAG + (tid & 7)] = (tid & 7) == 7 ? now() - 4000u : (tid & 7) == 6 ? ~0u : 0u;
  const rsrc rW = mkr(ka->ws, WS_END);
  BAR();
  const int xg = blockIdx.x & 7, r = blockIdx.x >> 3;
  const int h = xg * 4 + (r >> 3), b = (r >> 2) & 1, sp = r & 3, rg = xg * 6 + r / 5;
  const bool hasA = r >= 2, hasF = r < 30;
  const int g4 = lane >> 4, j = lane & 15, lb = (lane >> 3) & 1, e = lane & 7, a = lane >> 3;
  if (wv == 3) {
    lds_wait<2>(LU + F_POS, 8);
    const unsigned tf = __builtin_amdgcn_readfirstlane(LU[F_T1]);
    const unsigned d6 = __builtin_amdgcn_readfirstlane(LU[F_TB]) - tf + (hasA ? 0u : 110u);
    const unsigned tb = tf + (d6 < T_B ? d6 : T_B);
    LU[F_TB] = tb;
    lds_wait<2>(LU + F_A, 9);
    const unsigned epoch = LU[F_TAG];
    if (r == 2 && xg < 5) {
      until(tb + T_I);
#pragma nounroll
      for (int i = 0; i < 4; ++i) __builtin_amdgcn_raw_buffer_store_b128(v4u{0u, 0u, 0u, 0u}, rW, WS_AC + xg * 4096 + i * 1024 + lane * 16, 0, SC1);
      __builtin_amdgcn_raw_buffer_store_b64(v2u{0u, 0u}, mkr(ka->out, 2 * D * 2), (lane >> 5) * 1280 + xg * 256 + (lane & 31) * 8, 0, SC1);
      asm volatile("s_waitcnt vmcnt(0)" ::: "memory");
    }
    until(tb + T_Q);
    {
      const int o = WS_QP + b * QS + (48 * h + (lane < 48 ? lane : 0)) * 16;
      f4 p[5];
      gather<5, 8>(rW, {o, o, o, o, o}, {0, 2 * QS, 4 * QS, 6 * QS, 8 * QS}, epoch, p);
      const f4 s = ((p[0] + p[1]) + (p[2] + p[3])) + p[4];
      if (lane < 48) *(LAS v2u*)&LU[L_Q + 32 * g4 + 4 * e + 2 * lb] = v2u{pk(s.x, s.y), pk(s.z, s.w)};
      lds_post(LU + F_Q);
    }
    lds_wait<2>(LU + F_ST, 8);
    const f2 ml = *(LAS f2*)&L[L_ST + 2 * e];
    float M = max4(ml.x);
    M = fmaxf(M, dpp<HM>(M));
    const float ex = __builtin_amdgcn_exp2f(ml.x - M), Ls = sum8(ml.y * ex);
    L[L_FW + e] = ex / Ls;
    __builtin_amdgcn_raw_buffer_store_b64(v2u{asu(M + __builtin_amdgcn_logf(Ls)), epoch}, rW, lane < 16 ? WS_ST + (h * 8 + b * 4 + sp) * 128 + lane * 8 : WS_END, 0, SC1);
    until(now() + T_S);
    const int gq = 32 * rg + (lane & 31);
    const int own = WS_ST + (lane >= 32 ? (r % 5) * 32 + 2 : h * 8 + b * 4 + (lane & 3)) * 128;
    const int oth = hasF ? WS_ST + ((gq / 48) * 8 + (lane >> 5) * 4 + (lane & 3)) * 128 : own;
    v2u st;
#pragma nounroll
    for (unsigned spin = 0; spin < SPIN; ++spin) {
      MEMF();
      st = __builtin_amdgcn_raw_buffer_load_b64(rW, oth, 0, SC1);
      const v2u so = __builtin_amdgcn_raw_buffer_load_b64(rW, own, 0, SC1);
      if (!__any(((st.y ^ epoch) | (so.y ^ epoch)) != 0u)) break;
      __builtin_amdgcn_s_sleep(8);
    }
    float ws = __builtin_amdgcn_exp2f(asf(st.x) - max4(asf(st.x)));
    ws /= sum4(ws);
    f4 wgt = {dpp<0x00>(ws), dpp<0x55>(ws), dpp<0xAA>(ws), dpp<0xFF>(ws)};
    int o = WS_QP + (lane >> 5) * QS + gq * 16;
    int ao = L_O * 4 + (lane >> 5) * 320 + ((lane >> 3) & 3) * 64 + (lane & 3) * 16 + ((lane >> 2) & 1) * 8;
    asm volatile("" : "+v"(o), "+v"(ao), "+v"(wgt));
    BAR();
    if (hasF) {
      __builtin_amdgcn_s_sleep(10);
      lds_wait<1>(LU + F_W, N_W);
      f4 p[4];
      gather<4, 0>(rW, {o, o, o, o}, {0, 2 * QS, 4 * QS, 6 * QS}, epoch + STEP, p);
      const f4 s = (p[0] * wgt.x + p[1] * wgt.y) + (p[2] * wgt.z + p[3] * wgt.w);
      *(LAS v2u*)(LB + ao) = v2u{pk(s.x, s.y), pk(s.z, s.w)};
      BAR();
    }
    QUIT();
  } else {
    if (wv == 0) {
      const rsrc rX = mkr(ka->x, 2 * D * 2);
      const int jj = e | (g4 << 3);
      unsigned ep = __builtin_amdgcn_raw_buffer_load_b32(rW, WS_E, 0, SC1);
      const v4u xs0 = __builtin_amdgcn_raw_buffer_load_b128(rX, lb * 1280 + jj * 16, 0, 0);
      const v4u xs1 = __builtin_amdgcn_raw_buffer_load_b128(rX, lb * 1280 + 512 + jj * 16, 0, 0);
      const v2u xs2 = __builtin_amdgcn_raw_buffer_load_b64(rX, lb * 1280 + 1024 + jj * 8, 0, 0);
      SB();
      LU[F_TAG] = ep + STEP;
      *(LAS v4u*)(LB + L_XR * 4 + lb * 1280 + jj * 16) = xs0;
      *(LAS v4u*)(LB + L_XR * 4 + lb * 1280 + 512 + jj * 16) = xs1;
      *(LAS v2u*)(LB + L_XR * 4 + lb * 1280 + 1024 + jj * 8) = xs2;
      float v[20];
#pragma unroll
      for (int i = 0; i < 4; ++i) { v[2 * i] = bflo(xs0[i]); v[2 * i + 1] = bfhi(xs0[i]); v[8 + 2 * i] = bflo(xs1[i]); v[9 + 2 * i] = bfhi(xs1[i]); }
      v[16] = bflo(xs2[0]); v[17] = bfhi(xs2[0]); v[18] = bflo(xs2[1]); v[19] = bfhi(xs2[1]);
      float s = 0.f, q = 0.f;
#pragma unroll
      for (int i = 0; i < 20; ++i) s += v[i];
      const float mean = sum32of(s) * (1.f / D);
#pragma unroll
      for (int i = 0; i < 20; ++i) { const float d = v[i] - mean; q = fmaf(d, d, q); }
      const float mb = rbf(mean), rs = 1.f / rbf(sqrtf(rbf(rbf(sum32of(q) * (1.f / D)) + 1e-5f)));
#pragma unroll
      for (int i = 0; i < 5; ++i) {
        v4u o;
#pragma unroll
        for (int k = 0; k < 4; ++k) { const float t = rbf(v[4 * i + k] - mb) * rs; o[k] = pk(t, t); }
        *(LAS v4u*)&LU[L_XN + lb * 640 + (i < 4 ? (i >> 1) * 256 + 8 * jj + 4 * (i & 1) : 512 + 4 * jj)] = o;
      }
      lds_post(LU + F_A);
    }
    const int ua = hasA ? r - 2 : 0, ks = ua % 5, cp = ua / 5, row0 = 128 * ks + 16 * a;
    const unsigned half = (e & 1) ? 0xffff0000u : 0xffffu;
    const v4u msk = {(e >> 1) == 0 ? half : 0u, (e >> 1) == 1 ? half : 0u, (e >> 1) == 2 ? half : 0u, (e >> 1) == 3 ? half : 0u};
    lds_wait<2>(LU + F_A, 9);
    unsigned epoch = LU[F_TAG];
    const LAS v4u* const wa = (const LAS v4u*)&LU[L_A] + 1024 * wv + lane;
    f4 a0 = {0.f, 0.f, 0.f, 0.f}, a1 = a0;
#pragma nounroll
    for (int t4 = 0; t4 < 4; ++t4) {
      const v4u d0 = *(LAS v4u*)&LU[L_XN + row0 + 4 * t4], d1 = *(LAS v4u*)&LU[L_XN + 640 + row0 + 4 * t4];
#pragma unroll
      for (int k = 0; k < 4; ++k) {
        const v4u wt = wa[(4 * t4 + k) * 64];
        a0 = mfma(msk & d0[k], wt, a0);
        a1 = mfma(msk & d1[k], wt, a1);
      }
    }
    v4u y;
#pragma unroll
    for (int i = 0; i < 4; ++i) { const auto s2 = __builtin_amdgcn_permlane32_swap(asu(a0[i]), asu(a1[i]), false, false); y[i] = tagged(asf(s2[0]) + dpp<R8>(asf(s2[1])), epoch >> (8 * i)); }
    const int ab = cp < 4 ? WS_QP + ks * 2 * QS + 768 * (4 * xg + cp) : WS_GU + (ks * 4 + cp - 4) * GS + 768 * xg, as = cp < 4 ? QS : 2 * GS;
    __builtin_amdgcn_raw_buffer_store_b128(y, rW, lb == 0 && hasA ? ab + (lane >> 5) * as + (16 * wv + 8 * (g4 & 1) + e) * 16 : WS_END, 0, SC1);
    if (wv == 1 && hasF) {
      lds_wait<2>(LU + F_ST, 8);
      const int o = WS_GU + ((lane >> 5) * 12 + lb * 2 + (g4 & 1)) * GS + (16 * (rg >> 1) + 8 * (e >> 2) + 4 * (rg & 1) + (e & 3)) * 16;
      f4 p[3];
      gather<3, 8>(rW, {o, o, lane < 32 ? o + 8 * GS : o}, {0, 4 * GS, 0}, epoch, p);
      f4 s = p[0] + p[1];
      if (lane < 32) s += p[2];
      const auto sw = __builtin_amdgcn_permlane16_swap(asu(swadd32(s.x, s.y)), asu(swadd32(s.z, s.w)), false, false);
      const float g = rbf(asf(sw[0])), u = rbf(asf(sw[1]));
      const float z = 0.7978845608028654f * (g + 0.044715f * g * g * g);
      const float th = 1.f - 2.f / (1.f + __expf(2.f * z));
      const int fq = 2 * (e & 3) + (e >> 2);
      *(LAS u16*)(LB + L_O * 4 + lb * 320 + 256 + (fq & 3) * 16 + (fq >> 2) * 8 + (2 * (g4 & 1) + (g4 >> 1)) * 2) = (u16)pk(rbf(0.5f * g * (1.f + th)) * u, 0.f);
    }
    epoch = (epoch + STEP) >> (16 * (g4 & 1) + 8 * (g4 >> 1));
    int oo = WS_QP + (sp * 2 + b) * QS + (192 * h + 64 * wv + 4 * j + 2 * (g4 & 1) + (g4 >> 1)) * 4;
    int mo = (g4 * 4 * PST + 64 * wv + 4 * j) * 4, wo = (L_FW + 2 * g4) * 4;
    asm volatile("" : "+v"(oo), "+v"(mo), "+v"(wo), "+v"(epoch));
    asm volatile("s_branch 1f\n\t.p2align 12,,256\n1:");
    BAR();
    const f2 fa = *(LAS f2*)(LB + wo);
    f4 t[4];
#pragma unroll
    for (int i = 0; i < 4; ++i) t[i] = *(LAS f4*)(LB + mo + i * PST * 4);
    SB();
    const f4 s = (t[0] + t[1]) * fa.x + (t[2] + t[3]) * fa.y;
    __builtin_amdgcn_raw_buffer_store_b32(tagged(swadd16(swadd32(s.x, s.y), swadd32(s.z, s.w)), epoch), rW, oo, 0, SC1);
    QUIT();
  }
}
}

extern "C" void kernel_launch(void* const* d_in, const int* in_sizes, int n_in, void* d_out, int out_size, void* d_ws, size_t ws_size, hipStream_t stream) {
  fused<<<256, 768, 0, stream>>>((const u16*)d_in[0], (const u16*)d_in[1], (const u16*)d_in[2], (const u16*)d_in[3], (const u16*)d_in[4],
                                 (const u16*)d_in[6], (const u16*)d_in[7], (const u16*)d_in[8], (u16*)d_out, (unsigned char*)d_ws);
}
```
